```python
import torch

def _softmax(x, axis=-1):
    return torch.softmax(x, dim=axis)
def _gelu(x):
    return torch.nn.functional.gelu(x, approximate="tanh")
def _silu(x):
    return torch.nn.functional.silu(x)
def _sigmoid(x):
    return torch.sigmoid(x)
def _layernorm(x, axis=-1, eps=1e-5):
    m = x.mean(dim=axis, keepdim=True)
    v = x.var(dim=axis, keepdim=True, unbiased=False)
    return (x - m) / torch.sqrt(v + eps)
def _scatter_add(tgt, idx, src, axis=0):
    return tgt.clone().index_add_(axis, idx, src)
def _segment_sum(src, seg, n):
    out = torch.zeros((n,) + src.shape[1:], dtype=src.dtype)
    return out.index_add_(0, seg, src)
def _pad(x, pw):
    flat = tuple(v for pair in reversed(pw) for v in pair)
    return torch.nn.functional.pad(x, flat)

kernel_name = 'concat_heads__grouped_kv_mqa__compressed_kv_cla__prompt'

def setup_inputs():

    _w = (lambda a: (lambda t: t.to(torch.bfloat16) if t.is_floating_point() else t)(torch.as_tensor(a)))
    def _A_setup():
        B, S, D, H = 1, 2048, 512, 32
        Dh = D // H
        return {
            "x": _w((lambda *s: torch.randn(*s, dtype=torch.bfloat16) * 0.05)(B, S, D)),
            "wq": _w((lambda *s: torch.randn(*s, dtype=torch.bfloat16) * 0.8)(D, H * Dh)),
            "wk": _w((lambda *s: torch.randn(*s, dtype=torch.bfloat16) * 0.8)(D, Dh)),
            "wv": _w((lambda *s: torch.randn(*s, dtype=torch.bfloat16) * 0.05)(D, Dh)),
            "wo": _w((lambda *s: torch.randn(*s, dtype=torch.bfloat16) * 0.05)(H * Dh, D)),
        }
    _A_in = _A_setup()

    def _B_setup():
        B, H, Sq, Sk, D, M = 1, 32, 2048, 2048, 128, 512
        Dc = 4 * D
        return {
            "x": _w((lambda *s: torch.randn(*s, dtype=torch.bfloat16) * 3.2)(B, Sq, M)),
            "c_kv": _w((lambda *s: torch.randn(*s, dtype=torch.bfloat16) * 0.1)(B, Sk, Dc)),
            "w_q": _w((lambda *s: torch.randn(*s, dtype=torch.bfloat16) * 6.4)(M, H, D) * (M ** -0.5)),
            "w_uk": _w((lambda *s: torch.randn(*s, dtype=torch.bfloat16) * 6.4)(Dc, H, D) * (Dc ** -0.5)),
            "w_uv": _w((lambda *s: torch.randn(*s, dtype=torch.bfloat16) * 0.1)(Dc, H, D) * (Dc ** -0.5)),
            "w_o": _w((lambda *s: torch.randn(*s, dtype=torch.bfloat16) * 0.1)(H, D, M) * ((H * D) ** -0.5)),
        }
    _B_in = _B_setup()

    return {
        "A_x": _A_in["x"],
        "A_wq": _A_in["wq"],
        "A_wk": _A_in["wk"],
        "A_wv": _A_in["wv"],
        "A_wo": _A_in["wo"],
        "B_x": _B_in["x"],
        "B_c_kv": _B_in["c_kv"],
        "B_w_q": _B_in["w_q"],
        "B_w_uk": _B_in["w_uk"],
        "B_w_uv": _B_in["w_uv"],
        "B_w_o": _B_in["w_o"],
    }

def reference(A_x, A_wq, A_wk, A_wv, A_wo, B_x, B_c_kv, B_w_q, B_w_uk, B_w_uv, B_w_o):
    def _A_ref(x, wq, wk, wv, wo):
        B, S, D = x.shape
        H = 32
        Dh = D // H
        q = (lambda x, *d: x.permute(*d))((x @ wq).reshape(B, S, H, Dh), 0, 2, 1, 3)
        k = x @ wk
        v = x @ wv
        scores = torch.einsum("bhqd,bkd->bhqk", q, k) * (Dh ** -0.5)
        p = _softmax(scores, axis=-1)
        o = torch.einsum("bhqk,bkd->bhqd", p, v)
        return (lambda x, *d: x.permute(*d))(o, 0, 2, 1, 3).reshape(B, S, H * Dh) @ wo

    def _B_ref(x, c_kv, w_q, w_uk, w_uv, w_o):
        B, Sq, M = x.shape[0], x.shape[1], x.shape[2]
        Sk = c_kv.shape[1]
        H, D = w_q.shape[1], w_q.shape[2]
        q = torch.einsum("bqm,mhd->bhqd", x, w_q)
        k = torch.einsum("bkc,chd->bhkd", c_kv, w_uk)
        v = torch.einsum("bkc,chd->bhkd", c_kv, w_uv)
        scores = torch.einsum("bhqd,bhkd->bhqk", q, k) * (D ** -0.5)
        off = Sk - Sq
        qi = torch.arange(Sq)[:, None]
        kj = torch.arange(Sk)[None, :]
        cmask = kj <= (qi + off)
        scores = torch.where(cmask[None, None], scores, -1e9)
        p = _softmax(scores, axis=-1)
        ctx = torch.einsum("bhqk,bhkd->bhqd", p, v)
        return torch.einsum("bhqd,hdm->bqm", ctx, w_o)

    a = _A_ref(A_x, A_wq, A_wk, A_wv, A_wo)
    b = _B_ref(B_x, B_c_kv, B_w_q, B_w_uk, B_w_uv, B_w_o)
    return torch.concatenate([a, b], axis=-1)
```

```cpp
#include <hip/hip_runtime.h>
#include <hip/hip_bf16.h>
#include <cstdint>

using bf16 = __hip_bfloat16;
typedef short bf16x8 __attribute__((ext_vector_type(8)));
typedef short s16x4 __attribute__((ext_vector_type(4)));
typedef float f32x16 __attribute__((ext_vector_type(16)));
typedef float f32x4 __attribute__((ext_vector_type(4)));
typedef unsigned u32x4 __attribute__((ext_vector_type(4)));
typedef unsigned u32x2 __attribute__((ext_vector_type(2)));

constexpr int S = 2048, MD = 512, NH = 32, DB = 128, HD = NH * DB;
constexpr size_t WS_QB = 0, WS_KB = WS_QB + (size_t)S * HD * 2, WS_VB = WS_KB + (size_t)S * HD * 2, WS_CB = WS_VB + (size_t)S * HD * 2;
constexpr size_t WS_QA = WS_CB + (size_t)S * HD * 2, WS_CA = WS_QA + (size_t)S * MD * 2, WS_KA = WS_CA + (size_t)S * MD * 2, WS_VAT = WS_KA + (size_t)S * 16 * 2;

#define SBAR() __builtin_amdgcn_sched_barrier(0)
#define VMW() asm volatile("s_waitcnt vmcnt(0)" ::: "memory")
__device__ __forceinline__ int crow(int r, int hi) { return (r & 3) + 8 * (r >> 2) + 4 * hi; }
__device__ __forceinline__ unsigned cvtpk(float lo, float hi) {
    unsigned r; asm volatile("v_cvt_pk_bf16_f32 %0, %1, %2" : "=v"(r) : "v"(lo), "v"(hi)); return r;
}
template <int NC> __device__ __forceinline__ int v_st(int k, int c) { const int kk = (k & ~0xC) | ((k & 4) << 1) | ((k & 8) >> 1); return ((kk >> 3) * NC + (c >> 5)) * 512 + ((kk & 7) * 32 + (c & 31)) * 2; }
__device__ __forceinline__ int v_rd_base(int lane) { return ((lane & 3) << 3) | (((lane >> 2) & 3) << 6) | (((lane >> 4) & 1) << 5) | (((lane >> 5) & 1) << 8); }
#define TRRD(dst, base, off) asm volatile("ds_read_b64_tr_b16 %0, %1 offset:%2" : "=&v"(dst) : "v"(base), "i"(off) : "memory")
#define MK8(l, h) (bf16x8){l[0], l[1], l[2], l[3], h[0], h[1], h[2], h[3]}
#define LGKM0() do { asm volatile("s_waitcnt lgkmcnt(0)" ::: "memory"); SBAR(); } while (0)

constexpr int G1_TILES_B = 16 * 32;
__global__ __launch_bounds__(256, 2)
void proj_kernel(const bf16* __restrict__ Ax, const bf16* __restrict__ Awq, const bf16* __restrict__ Awk, const bf16* __restrict__ Awv,
                 const bf16* __restrict__ Bx, const bf16* __restrict__ Bc, const bf16* __restrict__ Bwq, const bf16* __restrict__ Bwuk,
                 const bf16* __restrict__ Bwuv, char* __restrict__ ws) {
    extern __shared__ __attribute__((aligned(16))) char lds[];
    const int tid = threadIdx.x, wid = tid >> 6, lane = tid & 63, r32 = lane & 31, hi = lane >> 5, wm = wid >> 1, wn = wid & 1;
    int id = blockIdx.x;
    const bf16* X; const bf16* W; bf16* C; int ldw, ldc, tm, tn, mode = 0;
    if (id < 3 * G1_TILES_B) { const int g = id / G1_TILES_B, t = id - g * G1_TILES_B;
        X = g == 0 ? Bx : Bc; W = g == 0 ? Bwq : (g == 1 ? Bwuk : Bwuv); C = (bf16*)(ws + (g == 0 ? WS_QB : (g == 1 ? WS_KB : WS_VB)));
        ldw = HD; ldc = HD; tm = t & 15; tn = t >> 4; }
    else if (id < 3 * G1_TILES_B + 64) { const int t = id - 3 * G1_TILES_B; X = Ax; W = Awq; C = (bf16*)(ws + WS_QA); ldw = MD; ldc = MD; tm = t & 15; tn = t >> 4; }
    else { const int t = id - 3 * G1_TILES_B - 64; X = Ax; W = Awk; C = nullptr; ldw = 16; ldc = 0; tm = t; tn = 0; mode = 1; }
    const int m0 = tm * 128, n0 = tn * 128;
    char* As = lds; char* Bs = lds + 2 * 16384;
    bf16x8 sa[4], sb[4];
    auto gload = [&](int kt) {
#pragma unroll
        for (int i = 0; i < 4; ++i) { const int c = tid + 256 * i, row = c >> 3, ch = c & 7;
            sa[i] = *(const bf16x8*)(X + (size_t)(m0 + row) * MD + kt * 64 + ch * 8); }
#pragma unroll
        for (int i = 0; i < 4; ++i) { const int c = tid + 256 * i, k = c >> 4, ch = c & 15;
            if (mode == 0) sb[i] = *(const bf16x8*)(W + (size_t)(kt * 64 + k) * ldw + n0 + ch * 8);
            else { bf16x8 z = {}; if (ch < 2) z = *(const bf16x8*)(Awk + (size_t)(kt * 64 + k) * 16 + ch * 8);
                   else if (ch < 4) z = *(const bf16x8*)(Awv + (size_t)(kt * 64 + k) * 16 + (ch - 2) * 8); sb[i] = z; } }
    };
    auto swrite = [&](int bufi) {
#pragma unroll
        for (int i = 0; i < 4; ++i) { const int c = tid + 256 * i, row = c >> 3, ch = c & 7;
            *(bf16x8*)(As + bufi * 16384 + row * 128 + ((ch ^ ((row >> 1) & 7)) << 4)) = sa[i]; }
#pragma unroll
        for (int i = 0; i < 4; ++i) { const int c = tid + 256 * i, k = c >> 4, ch = c & 15;
            *(bf16x8*)(Bs + bufi * 16384 + v_st<4>(k, ch * 8)) = sb[i]; }
    };
    f32x16 acc[2][2] = {};
    gload(0); VMW(); swrite(0); __syncthreads();
    const int arow0 = wm * 64 + r32;
    const int vb = (int)(uintptr_t)Bs + v_rd_base(lane) + wn * 1024;
#pragma unroll 1
    for (int kt = 0; kt < 8; ++kt) {
        const int cur = kt & 1;
        if (kt + 1 < 8) gload(kt + 1);
        const char* Ab = As + cur * 16384; const int vbb = vb + cur * 16384;
#pragma unroll
        for (int ks = 0; ks < 4; ++ks) {
            bf16x8 a0 = *(const bf16x8*)(Ab + arow0 * 128 + (((ks * 2 + hi) ^ ((arow0 >> 1) & 7)) << 4));
            bf16x8 a1 = *(const bf16x8*)(Ab + (arow0 + 32) * 128 + (((ks * 2 + hi) ^ ((arow0 >> 1) & 7)) << 4));
            s16x4 l0, h0, l1, h1;
            TRRD(l0, vbb, ks * 4096); TRRD(h0, vbb, ks * 4096 + 2048); TRRD(l1, vbb, ks * 4096 + 512); TRRD(h1, vbb, ks * 4096 + 2048 + 512);
            LGKM0();
            const bf16x8 b0 = MK8(l0, h0), b1 = MK8(l1, h1);
            acc[0][0] = __builtin_amdgcn_mfma_f32_32x32x16_bf16(b0, a0, acc[0][0], 0, 0, 0);
            acc[0][1] = __builtin_amdgcn_mfma_f32_32x32x16_bf16(b1, a0, acc[0][1], 0, 0, 0);
            acc[1][0] = __builtin_amdgcn_mfma_f32_32x32x16_bf16(b0, a1, acc[1][0], 0, 0, 0);
            acc[1][1] = __builtin_amdgcn_mfma_f32_32x32x16_bf16(b1, a1, acc[1][1], 0, 0, 0);
        }
        if (kt + 1 < 8) { VMW(); swrite(cur ^ 1); }
        __syncthreads();
    }
    char* Cs = lds;
#pragma unroll
    for (int mt = 0; mt < 2; ++mt)
#pragma unroll
        for (int nt = 0; nt < 2; ++nt)
#pragma unroll
            for (int g = 0; g < 4; ++g) { const int m = wm * 64 + mt * 32 + r32, n = wn * 64 + nt * 32 + 8 * g + 4 * hi;
                u32x2 w = {cvtpk(acc[mt][nt][4 * g], acc[mt][nt][4 * g + 1]), cvtpk(acc[mt][nt][4 * g + 2], acc[mt][nt][4 * g + 3])};
                *(u32x2*)(Cs + m * 272 + n * 2) = w; }
    __syncthreads();
    if (mode == 0) {
#pragma unroll
        for (int i = 0; i < 8; ++i) { const int c = tid + 256 * i, row = c >> 4, ch = c & 15;
            *(bf16x8*)(C + (size_t)(m0 + row) * ldc + n0 + ch * 8) = *(const bf16x8*)(Cs + row * 272 + ch * 16); }
    } else {
        bf16* KA = (bf16*)(ws + WS_KA); bf16* VAT = (bf16*)(ws + WS_VAT);
        { const int row = tid >> 1, ch = tid & 1; *(bf16x8*)(KA + (size_t)(m0 + row) * 16 + ch * 8) = *(const bf16x8*)(Cs + row * 272 + ch * 16); }
#pragma unroll
        for (int i = 0; i < 8; ++i) { const int c = tid + 256 * i, d = c >> 7, row = c & 127;
            VAT[(size_t)d * S + m0 + row] = *(const bf16*)(Cs + row * 272 + 32 + d * 2); }
    }
}

constexpr float SCALE_B = 0.08838834764831845f, SCALE_A = 0.25f, THR = 8.f, LOG2E = 1.4426950408889634f;
constexpr int NW = 4, QBLK = 32, KVBLK = 64, QB = NW * QBLK;
constexpr int SHM_V = KVBLK * DB * 2, SHM_K = KVBLK * DB * 2;
constexpr int LDS_ATT = 2 * SHM_V + 2 * SHM_K + NW * 64 * 4;
#define KSWZ(row, colB) ((row) * 256 + ((colB) ^ (((row) & 7) << 4)))

__device__ __forceinline__ void mask_tile(f32x16& p0, f32x16& p1, int dq) {
    const float NEG = -__builtin_inff();
#pragma unroll
    for (int r = 0; r < 16; ++r) { const int c = (r & 3) + 8 * (r >> 2);
        if (dq - c < 0) p0[r] = NEG;
        if (dq - c - 32 < 0) p1[r] = NEG; }
}
__device__ __forceinline__ void partialSM(f32x16& p0, f32x16& p1, float& m_reg, float& mn, float& alpha) {
    float pmax = p0[0]; for (int r = 1; r < 16; ++r) pmax = fmaxf(pmax, p0[r]); for (int r = 0; r < 16; ++r) pmax = fmaxf(pmax, p1[r]);
    { auto rr = __builtin_amdgcn_permlane32_swap(__float_as_uint(pmax), __float_as_uint(pmax), false, false);
      pmax = fmaxf(__uint_as_float(rr[0]), __uint_as_float(rr[1])); }
    constexpr float C2 = LOG2E * SCALE_B;
    if (__builtin_expect(__all((pmax - m_reg) * SCALE_B <= THR), 1)) { mn = m_reg; alpha = 1.f; }
    else { mn = fmaxf(m_reg, pmax); alpha = __builtin_amdgcn_exp2f((m_reg - mn) * C2); m_reg = mn; }
    const float mnL = -mn * C2;
    for (int r = 0; r < 16; ++r) p0[r] = fmaf(p0[r], C2, mnL); for (int r = 0; r < 16; ++r) p1[r] = fmaf(p1[r], C2, mnL);
    for (int r = 0; r < 16; ++r) p0[r] = __builtin_amdgcn_exp2f(p0[r]);
}
__device__ __forceinline__ void finishSM(f32x16& p0, f32x16& p1, float alpha, float& l_reg, bf16x8& pa0, bf16x8& pa1, bf16x8& pa2, bf16x8& pa3) {
    for (int r = 0; r < 16; ++r) p1[r] = __builtin_amdgcn_exp2f(p1[r]);
    float ps = 0; for (int r = 0; r < 16; ++r) ps += p0[r]; for (int r = 0; r < 16; ++r) ps += p1[r];
    { auto rr = __builtin_amdgcn_permlane32_swap(__float_as_uint(ps), __float_as_uint(ps), false, false);
      ps = __uint_as_float(rr[0]) + __uint_as_float(rr[1]); }
    l_reg = l_reg * alpha + ps;
#define PK4(P, B_, OUT) do { unsigned a0 = cvtpk(P[B_+0], P[B_+1]), a1 = cvtpk(P[B_+2], P[B_+3]);                          \
        unsigned b0 = cvtpk(P[B_+4], P[B_+5]), b1 = cvtpk(P[B_+6], P[B_+7]);                                             \
        auto r0 = __builtin_amdgcn_permlane32_swap(a0, b0, false, false); auto r1 = __builtin_amdgcn_permlane32_swap(a1, b1, false, false); \
        u32x4 w = {r0[0], r1[0], r0[1], r1[1]}; OUT = *reinterpret_cast<bf16x8*>(&w); } while (0)
    PK4(p0, 0, pa0); PK4(p0, 8, pa1); PK4(p1, 0, pa2); PK4(p1, 8, pa3);
#undef PK4
}
template <int KB>
__device__ __forceinline__ void qkt(f32x16& p0, f32x16& p1, const char* K_lds, int r32, int hi, const bf16x8* qr) {
    p0 = f32x16{}; p1 = f32x16{};
    const char* kb[4];
#pragma unroll
    for (int dd = 0; dd < 4; ++dd) kb[dd] = K_lds + KB * SHM_K + KSWZ(r32, (dd * 16 + hi * 8) * 2);
#pragma unroll
    for (int d0 = 0; d0 < 8; ++d0) { const char* a = kb[d0 & 3] + (d0 >> 2) * 128;
        bf16x8 b0 = *reinterpret_cast<const bf16x8*>(a);
        bf16x8 b1 = *reinterpret_cast<const bf16x8*>(a + 32 * 256);
        p0 = __builtin_amdgcn_mfma_f32_32x32x16_bf16(b0, qr[d0], p0, 0, 0, 0);
        p1 = __builtin_amdgcn_mfma_f32_32x32x16_bf16(b1, qr[d0], p1, 0, 0, 0); }
}
template <int VB>
__device__ __forceinline__ void pv_tile(f32x16* o, int vb0, bf16x8 pa0, bf16x8 pa1, bf16x8 pa2, bf16x8 pa3) {
#define PV_D0(d0) do { s16x4 l0, l1, l2, l3, h0, h1, h2, h3; constexpr int b_ = VB * SHM_V + (d0) * 512;   \
        TRRD(l0, vb0, b_); TRRD(h0, vb0, b_ + 2048); TRRD(l1, vb0, b_ + 4096); TRRD(h1, vb0, b_ + 6144); TRRD(l2, vb0, b_ + 8192); TRRD(h2, vb0, b_ + 10240); TRRD(l3, vb0, b_ + 12288); TRRD(h3, vb0, b_ + 14336); \
        LGKM0();             \
        o[d0] = __builtin_amdgcn_mfma_f32_32x32x16_bf16(pa0, MK8(l0, h0), o[d0], 0, 0, 0);   \
        o[d0] = __builtin_amdgcn_mfma_f32_32x32x16_bf16(pa1, MK8(l1, h1), o[d0], 0, 0, 0);   \
        o[d0] = __builtin_amdgcn_mfma_f32_32x32x16_bf16(pa2, MK8(l2, h2), o[d0], 0, 0, 0);   \
        o[d0] = __builtin_amdgcn_mfma_f32_32x32x16_bf16(pa3, MK8(l3, h3), o[d0], 0, 0, 0); } while (0)
    PV_D0(0); PV_D0(1); PV_D0(2); PV_D0(3);
#undef PV_D0
}
struct Seam { bf16x8 qr[8]; bf16x8 sv[4]; };
__device__ __forceinline__ void k_dma(const bf16* Kh, int k0, char* K_lds, int bufi, int wid, unsigned koff) {
#pragma unroll
    for (int i = 0; i < 4; ++i) { const char* ub = (const char*)Kh + (size_t)(k0 + 16 * i) * (HD * 2);
        __builtin_amdgcn_global_load_lds((const unsigned*)(ub + koff), (unsigned*)(K_lds + bufi * SHM_K + i * 4096 + wid * 1024), 16, 0, 0); }
}
#define VLOAD(Vp, k0) do { _Pragma("unroll") for (int i_ = 0; i_ < 4; ++i_) { const char* ub_ = (const char*)(Vp) + (size_t)((k0) + 16 * i_) * (HD * 2); Sm.sv[i_] = *(const bf16x8*)(ub_ + voff); } } while (0)
#define VWRITE(bf) do { _Pragma("unroll") for (int i_ = 0; i_ < 4; ++i_) *(bf16x8*)(V_lds + (bf) * SHM_V + vst + i_ * 4096) = Sm.sv[i_]; } while (0)

__device__ __forceinline__ void attnB_block(const bf16* Qh, const bf16* Kh, const bf16* Vh, bf16* Oh, int P0, int P0n, char* lds, Seam& Sm) {
    const int tid = threadIdx.x, wid = __builtin_amdgcn_readfirstlane(tid >> 6), lane = tid & 63, r32 = lane & 31, hi = lane >> 5;
    const int NT = (P0 + QB) / KVBLK;
    const int qlo = P0 + wid * QBLK, qm = qlo + r32 - 4 * hi;
    char* V_lds = lds; char* K_lds = lds + 2 * SHM_V;
    float* wsf = (float*)(lds + 2 * SHM_V + 2 * SHM_K) + wid * 64; float* li_l = wsf, * al_l = wsf + 32;
    float m_reg = -1e30f, l_reg = 0; f32x16 o[4] = {};
    const int sr = tid >> 4, sc = (tid & 15) * 8, vst = v_st<4>(sr, sc);
    const int vb0 = (int)(uintptr_t)V_lds + v_rd_base(lane);
    const unsigned krow = wid * 4 + (lane >> 4), koff = (krow * HD + (((lane & 15) ^ (krow & 7)) * 8)) * 2, voff = (sr * HD + sc) * 2;
#define RESC(a) do { if (__any((a) < 1.f)) { if (hi == 0) al_l[r32] = (a); asm volatile("s_waitcnt lgkmcnt(0)" ::: "memory");              \
                     for (int d_ = 0; d_ < 4; ++d_) for (int r = 0; r < 16; ++r) o[d_][r] *= al_l[crow(r, hi)]; } } while (0)
#define MASKT(P0_, P1_, t) do { const int kb_ = (t) * KVBLK; if (kb_ + KVBLK - 1 > qlo) mask_tile(P0_, P1_, qm - kb_); } while (0)
    f32x16 pA0, pA1, pB0, pB1; float mnA, mnB, alA, alB; bf16x8 pa0, pa1, pa2, pa3;
    VWRITE(0); SBAR();
    VLOAD(Vh, KVBLK); k_dma(Kh, KVBLK, K_lds, 1, wid, koff);
    SBAR(); qkt<0>(pA0, pA1, K_lds, r32, hi, Sm.qr);
    MASKT(pA0, pA1, 0); partialSM(pA0, pA1, m_reg, mnA, alA);
    VMW(); VWRITE(1);
    __syncthreads();
#define HALF_STEP(PX0, PX1, mnX, alX, PY0, PY1, alY, t, KB, VB, SB) do {                                                      \
        SBAR(); qkt<KB>(PX0, PX1, K_lds, r32, hi, Sm.qr);                                                                     \
        finishSM(PY0, PY1, alY, l_reg, pa0, pa1, pa2, pa3); SBAR();                                                           \
        VLOAD(Vh, ((t) + 1) * KVBLK); k_dma(Kh, ((t) + 1) * KVBLK, K_lds, SB, wid, koff); SBAR();                             \
        pv_tile<VB>(o, vb0, pa0, pa1, pa2, pa3); MASKT(PX0, PX1, (t)); partialSM(PX0, PX1, m_reg, mnX, alX);                  \
        __syncthreads();                                                                                                      \
        VMW(); VWRITE(SB);                                                                                                    \
        RESC(alX); __syncthreads(); } while (0)
    for (int t = 1; t + 1 < NT; t += 2) {
        HALF_STEP(pB0, pB1, mnB, alB, pA0, pA1, alA, t, 1, 0, 0);
        HALF_STEP(pA0, pA1, mnA, alA, pB0, pB1, alB, t + 1, 0, 1, 1);
    }
    SBAR(); qkt<1>(pB0, pB1, K_lds, r32, hi, Sm.qr); SBAR();
    VLOAD(Vh, 0); SBAR();
    {   int lz = lane; asm volatile("" : "+v"(lz));
        const unsigned qoff = ((lz & 31) * HD + (lz >> 5) * 8) * 2; const char* ub = (const char*)Qh + (size_t)(P0n + wid * QBLK) * (HD * 2);
#pragma unroll
        for (int d0 = 0; d0 < 8; ++d0) Sm.qr[d0] = *(const bf16x8*)(ub + qoff + d0 * 32); }
    SBAR();
    finishSM(pA0, pA1, alA, l_reg, pa0, pa1, pa2, pa3); SBAR();
    pv_tile<0>(o, vb0, pa0, pa1, pa2, pa3);
    MASKT(pB0, pB1, NT - 1); partialSM(pB0, pB1, m_reg, mnB, alB); __syncthreads();
    k_dma(Kh, 0, K_lds, 0, wid, koff);
    RESC(alB);
    finishSM(pB0, pB1, alB, l_reg, pa0, pa1, pa2, pa3); SBAR(); pv_tile<1>(o, vb0, pa0, pa1, pa2, pa3);
    SBAR();
    if (hi == 0) li_l[r32] = l_reg; asm volatile("s_waitcnt lgkmcnt(0)" ::: "memory");
    float rli[16];
#pragma unroll
    for (int r = 0; r < 16; ++r) rli[r] = __builtin_amdgcn_rcpf(li_l[crow(r, hi)]);
    char* Ow = (char*)(Oh + (size_t)(P0 + wid * QBLK) * HD);
    {   int lz = lane; asm volatile("" : "+v"(lz));
        const unsigned ooff = ((lz >> 5) * 4 * HD + (lz & 31)) * 2;
#pragma unroll
        for (int r = 0; r < 16; ++r) { const int orow = (r & 3) + 8 * (r >> 2);
#pragma unroll
            for (int d0 = 0; d0 < 4; ++d0) { const float v = o[d0][r] * rli[r]; const float vn = __shfl_xor(v, 1);
                if ((lz & 1) == 0) *(unsigned*)(Ow + (size_t)(orow * HD * 2 + d0 * 64) + ooff) = cvtpk(v, vn); } } }
    VMW();
    __syncthreads();
#undef RESC
#undef MASKT
#undef HALF_STEP
}

__device__ __forceinline__ void attnA_block(int a, const char* ws) {
    const int tid = threadIdx.x, wid = tid >> 6, lane = tid & 63, r32 = lane & 31, hi = lane >> 5;
    const int h = a >> 3, q0 = (a & 7) * 256 + wid * 64;
    const bf16* QA = (const bf16*)(ws + WS_QA); const bf16* KA = (const bf16*)(ws + WS_KA); const bf16* VAT = (const bf16*)(ws + WS_VAT);
    bf16* CA = (bf16*)(ws + WS_CA);
    bf16x8 qf[2];
#pragma unroll
    for (int s = 0; s < 2; ++s) qf[s] = *(const bf16x8*)(QA + (size_t)(q0 + s * 32 + r32) * MD + h * 16 + hi * 8);
    f32x16 o[2] = {}; float m_reg[2] = {-1e30f, -1e30f};
    constexpr float C2 = LOG2E * SCALE_A;
    const bf16* kp = KA + (size_t)r32 * 16 + hi * 8;
    const bf16* vp = VAT + (size_t)(r32 & 15) * S + 4 * hi;
    const bool vrow = r32 < 16;
    const short one = (r32 == 16) ? (short)0x3F80 : (short)0;
    bf16x8 kf = *(const bf16x8*)(kp);
    s16x4 v00 = *(const s16x4*)(vp), v01 = *(const s16x4*)(vp + 8), v10 = *(const s16x4*)(vp + 16), v11 = *(const s16x4*)(vp + 24);
#pragma unroll 1
    for (int k0 = 0; k0 < S; k0 += 32) {
        const bf16x8 kc = kf; bf16x8 va0, va1;
        { const s16x4 o4 = {one, one, one, one};
          const s16x4 x00 = vrow ? v00 : o4, x01 = vrow ? v01 : o4, x10 = vrow ? v10 : o4, x11 = vrow ? v11 : o4;
          va0 = MK8(x00, x01); va1 = MK8(x10, x11); }
        const int kn = (k0 + 32 < S) ? k0 + 32 : k0;
        kf = *(const bf16x8*)(kp + (size_t)kn * 16);
        v00 = *(const s16x4*)(vp + kn); v01 = *(const s16x4*)(vp + kn + 8); v10 = *(const s16x4*)(vp + kn + 16); v11 = *(const s16x4*)(vp + kn + 24);
        f32x16 p[2];
#pragma unroll
        for (int s = 0; s < 2; ++s) p[s] = __builtin_amdgcn_mfma_f32_32x32x16_bf16(kc, qf[s], f32x16{}, 0, 0, 0);
#pragma unroll
        for (int s = 0; s < 2; ++s) {
            float pmax = p[s][0];
#pragma unroll
            for (int r = 1; r < 16; ++r) pmax = fmaxf(pmax, p[s][r]);
            { auto rr = __builtin_amdgcn_permlane32_swap(__float_as_uint(pmax), __float_as_uint(pmax), false, false);
              pmax = fmaxf(__uint_as_float(rr[0]), __uint_as_float(rr[1])); }
            if (!__builtin_expect(__all((pmax - m_reg[s]) * SCALE_A <= THR), 1)) {
                const float mn = fmaxf(m_reg[s], pmax), alpha = __builtin_amdgcn_exp2f((m_reg[s] - mn) * C2); m_reg[s] = mn;
#pragma unroll
                for (int r = 0; r < 9; ++r) o[s][r] *= alpha;
            }
            const float mnL = -m_reg[s] * C2;
#pragma unroll
            for (int r = 0; r < 16; ++r) p[s][r] = __builtin_amdgcn_exp2f(fmaf(p[s][r], C2, mnL));
            u32x4 w0 = {cvtpk(p[s][0], p[s][1]), cvtpk(p[s][2], p[s][3]), cvtpk(p[s][4], p[s][5]), cvtpk(p[s][6], p[s][7])};
            u32x4 w1 = {cvtpk(p[s][8], p[s][9]), cvtpk(p[s][10], p[s][11]), cvtpk(p[s][12], p[s][13]), cvtpk(p[s][14], p[s][15])};
            o[s] = __builtin_amdgcn_mfma_f32_32x32x16_bf16(va0, *reinterpret_cast<bf16x8*>(&w0), o[s], 0, 0, 0);
            o[s] = __builtin_amdgcn_mfma_f32_32x32x16_bf16(va1, *reinterpret_cast<bf16x8*>(&w1), o[s], 0, 0, 0);
        }
    }
#pragma unroll
    for (int s = 0; s < 2; ++s) {
        float l = o[s][8];
        { auto rr = __builtin_amdgcn_permlane32_swap(__float_as_uint(l), __float_as_uint(l), false, false); l = __uint_as_float(rr[0]); }
        const float rl = 1.f / l;
        bf16* op = CA + (size_t)(q0 + s * 32 + r32) * MD + h * 16 + 4 * hi;
        u32x2 w0 = {cvtpk(o[s][0] * rl, o[s][1] * rl), cvtpk(o[s][2] * rl, o[s][3] * rl)};
        u32x2 w1 = {cvtpk(o[s][4] * rl, o[s][5] * rl), cvtpk(o[s][6] * rl, o[s][7] * rl)};
        *(u32x2*)op = w0; *(u32x2*)(op + 8) = w1;
    }
}

__global__ __launch_bounds__(256, 2)
void attn_kernel(char* __restrict__ ws) {
    extern __shared__ __attribute__((aligned(16))) char lds[];
    const int id = blockIdx.x;
    if (id >= 256) { attnA_block(id - 256, ws); return; }
    const int h = id >> 3, x = id & 7;
    const bf16* Qh = (const bf16*)(ws + WS_QB) + h * DB; const bf16* Kh = (const bf16*)(ws + WS_KB) + h * DB;
    const bf16* Vh = (const bf16*)(ws + WS_VB) + h * DB; bf16* Oh = (bf16*)(ws + WS_CB) + h * DB;
    const int Pa = (15 - x) * QB, Pb = x * QB;
    Seam Sm;
    {   const int tid = threadIdx.x, wid = __builtin_amdgcn_readfirstlane(tid >> 6), lane = tid & 63, r32 = lane & 31, hi = lane >> 5;
        const int sr = tid >> 4, sc = (tid & 15) * 8;
        const unsigned krow = wid * 4 + (lane >> 4), koff = (krow * HD + (((lane & 15) ^ (krow & 7)) * 8)) * 2, voff = (sr * HD + sc) * 2;
#pragma unroll
        for (int d0 = 0; d0 < 8; ++d0) Sm.qr[d0] = *(const bf16x8*)(Qh + (size_t)(Pa + wid * QBLK + r32) * HD + d0 * 16 + hi * 8);
        VLOAD(Vh, 0); k_dma(Kh, 0, lds + 2 * SHM_V, 0, wid, koff); VMW(); __syncthreads(); }
#pragma unroll 1
    for (int pass = 0; pass < 2; ++pass) attnB_block(Qh, Kh, Vh, Oh, pass ? Pb : Pa, Pb, lds, Sm);
}

__global__ __launch_bounds__(256, 1)
void outproj_kernel(const char* __restrict__ ws, const bf16* __restrict__ Awo, const bf16* __restrict__ Bwo, bf16* __restrict__ out) {
    extern __shared__ __attribute__((aligned(16))) char lds[];
    const int tid = threadIdx.x, wid = tid >> 6, lane = tid & 63, r32 = lane & 31, hi = lane >> 5;
    int id = blockIdx.x; const bool isB = id < 256; if (!isB) id -= 256;
    const bf16* X = (const bf16*)(ws + (isB ? WS_CB : WS_CA)); const bf16* W = isB ? Bwo : Awo;
    const int ldx = isB ? HD : MD, nst = isB ? 16 : 2, m0 = (id >> 3) * 64, n0 = (id & 7) * 64, oc = isB ? 512 : 0;
    char* As = lds; char* Bs = lds + 2 * 32768;
    bf16x8 sa[8], sb[8];
    auto gload = [&](int st) {
#pragma unroll
        for (int i = 0; i < 8; ++i) { const int c = tid + 256 * i, row = c >> 5, ch = c & 31;
            sa[i] = *(const bf16x8*)(X + (size_t)(m0 + row) * ldx + st * 256 + ch * 8); }
#pragma unroll
        for (int i = 0; i < 8; ++i) { const int c = tid + 256 * i, k = c >> 3, ch = c & 7;
            sb[i] = *(const bf16x8*)(W + (size_t)(st * 256 + k) * MD + n0 + ch * 8); }
    };
    auto swrite = [&](int bufi) {
#pragma unroll
        for (int i = 0; i < 8; ++i) { const int c = tid + 256 * i, row = c >> 5, ch = c & 31;
            *(bf16x8*)(As + bufi * 32768 + row * 512 + ((ch ^ (row & 15)) << 4)) = sa[i]; }
#pragma unroll
        for (int i = 0; i < 8; ++i) { const int c = tid + 256 * i, k = c >> 3, ch = c & 7;
            *(bf16x8*)(Bs + bufi * 32768 + v_st<2>(k, ch * 8)) = sb[i]; }
    };
    f32x16 acc[2][2] = {};
    gload(0); VMW(); swrite(0); __syncthreads();
    const int vb = (int)(uintptr_t)Bs + v_rd_base(lane) + wid * 8192;
#pragma unroll 1
    for (int st = 0; st < nst; ++st) {
        const int cur = st & 1;
        if (st + 1 < nst) gload(st + 1);
        const char* Ab = As + cur * 32768; const int vbb = vb + cur * 32768;
#pragma unroll
        for (int ks = 0; ks < 4; ++ks) {
            const int ch = wid * 8 + ks * 2 + hi;
            bf16x8 a0 = *(const bf16x8*)(Ab + r32 * 512 + ((ch ^ (r32 & 15)) << 4));
            bf16x8 a1 = *(const bf16x8*)(Ab + (r32 + 32) * 512 + ((ch ^ (r32 & 15)) << 4));
            s16x4 l0, h0, l1, h1;
            TRRD(l0, vbb, ks * 2048); TRRD(h0, vbb, ks * 2048 + 1024); TRRD(l1, vbb, ks * 2048 + 512); TRRD(h1, vbb, ks * 2048 + 1024 + 512);
            LGKM0();
            const bf16x8 b0 = MK8(l0, h0), b1 = MK8(l1, h1);
            acc[0][0] = __builtin_amdgcn_mfma_f32_32x32x16_bf16(b0, a0, acc[0][0], 0, 0, 0);
            acc[0][1] = __builtin_amdgcn_mfma_f32_32x32x16_bf16(b1, a0, acc[0][1], 0, 0, 0);
            acc[1][0] = __builtin_amdgcn_mfma_f32_32x32x16_bf16(b0, a1, acc[1][0], 0, 0, 0);
            acc[1][1] = __builtin_amdgcn_mfma_f32_32x32x16_bf16(b1, a1, acc[1][1], 0, 0, 0);
        }
        if (st + 1 < nst) { VMW(); swrite(cur ^ 1); }
        __syncthreads();
    }
    float* Rs = (float*)lds;
#pragma unroll
    for (int mt = 0; mt < 2; ++mt)
#pragma unroll
        for (int nt = 0; nt < 2; ++nt)
#pragma unroll
            for (int g = 0; g < 4; ++g) { const int m = mt * 32 + r32, n = nt * 32 + 8 * g + 4 * hi;
                f32x4 w = {acc[mt][nt][4 * g], acc[mt][nt][4 * g + 1], acc[mt][nt][4 * g + 2], acc[mt][nt][4 * g + 3]};
                *(f32x4*)(Rs + (wid * 64 + m) * 68 + n) = w; }
    __syncthreads();
#pragma unroll
    for (int i = 0; i < 2; ++i) { const int c = tid + 256 * i, row = c >> 3, ch = c & 7;
        f32x4 s0 = {}, s1 = {};
#pragma unroll
        for (int w = 0; w < 4; ++w) { s0 += *(const f32x4*)(Rs + (w * 64 + row) * 68 + ch * 8); s1 += *(const f32x4*)(Rs + (w * 64 + row) * 68 + ch * 8 + 4); }
        u32x4 o = {cvtpk(s0[0], s0[1]), cvtpk(s0[2], s0[3]), cvtpk(s1[0], s1[1]), cvtpk(s1[2], s1[3])};
        *(u32x4*)(out + (size_t)(m0 + row) * 1024 + oc + n0 + ch * 8) = o; }
}

extern "C" void kernel_launch(void* const* d_in, const int* in_sizes, int n_in, void* d_out, int out_size, void* d_ws, size_t ws_size,
                              hipStream_t stream) {
    (void)in_sizes; (void)n_in; (void)out_size; (void)ws_size;
    (void)hipFuncSetAttribute((const void*)attn_kernel, hipFuncAttributeMaxDynamicSharedMemorySize, LDS_ATT);
    (void)hipFuncSetAttribute((const void*)outproj_kernel, hipFuncAttributeMaxDynamicSharedMemorySize, 131072);
    (void)hipFuncSetAttribute((const void*)proj_kernel, hipFuncAttributeMaxDynamicSharedMemorySize, 65536);
    char* ws = (char*)d_ws;
    proj_kernel<<<3 * G1_TILES_B + 64 + 16, 256, 65536, stream>>>((const bf16*)d_in[0], (const bf16*)d_in[1], (const bf16*)d_in[2], (const bf16*)d_in[3],
        (const bf16*)d_in[5], (const bf16*)d_in[6], (const bf16*)d_in[7], (const bf16*)d_in[8], (const bf16*)d_in[9], ws);
    attn_kernel<<<512, 256, LDS_ATT, stream>>>(ws);
    outproj_kernel<<<512, 256, 131072, stream>>>(ws, (const bf16*)d_in[4], (const bf16*)d_in[10], (bf16*)d_out);
}
```

```cpp
#include <hip/hip_runtime.h>
#include <hip/hip_bf16.h>
#include <cstdint>

using bf16 = __hip_bfloat16;
typedef short bf16x8 __attribute__((ext_vector_type(8)));
typedef short s16x4 __attribute__((ext_vector_type(4)));
typedef float f32x16 __attribute__((ext_vector_type(16)));
typedef float f32x4 __attribute__((ext_vector_type(4)));
typedef unsigned u32x4 __attribute__((ext_vector_type(4)));
typedef unsigned u32x2 __attribute__((ext_vector_type(2)));

constexpr int S = 2048, MD = 512, NH = 32, DB = 128, HD = NH * DB;
constexpr size_t WS_QB = 0, WS_KB = WS_QB + (size_t)S * HD * 2, WS_VB = WS_KB + (size_t)S * HD * 2, WS_CB = WS_VB + (size_t)S * HD * 2;
constexpr size_t WS_QA = WS_CB + (size_t)S * HD * 2, WS_CA = WS_QA + (size_t)S * MD * 2, WS_KA = WS_CA + (size_t)S * MD * 2, WS_VAT = WS_KA + (size_t)S * 16 * 2, WS_PART = WS_VAT + (size_t)S * 16 * 2;

#define SBAR() __builtin_amdgcn_sched_barrier(0)
#define VMW() asm volatile("s_waitcnt vmcnt(0)" ::: "memory")
__device__ __forceinline__ int crow(int r, int hi) { return (r & 3) + 8 * (r >> 2) + 4 * hi; }
__device__ __forceinline__ unsigned cvtpk(float lo, float hi) {
    unsigned r; asm volatile("v_cvt_pk_bf16_f32 %0, %1, %2" : "=v"(r) : "v"(lo), "v"(hi)); return r;
}
template <int NC> __device__ __forceinline__ int v_st(int k, int c) { const int kk = (k & ~0xC) | ((k & 4) << 1) | ((k & 8) >> 1); return ((kk >> 3) * NC + (c >> 5)) * 512 + ((kk & 7) * 32 + (c & 31)) * 2; }
__device__ __forceinline__ int v_rd_base(int lane) { return ((lane & 3) << 3) | (((lane >> 2) & 3) << 6) | (((lane >> 4) & 1) << 5) | (((lane >> 5) & 1) << 8); }
#define TRRD(dst, base, off) asm volatile("ds_read_b64_tr_b16 %0, %1 offset:%2" : "=&v"(dst) : "v"(base), "i"(off) : "memory")
#define MK8(l, h) (bf16x8){l[0], l[1], l[2], l[3], h[0], h[1], h[2], h[3]}
#define LGKM0() do { asm volatile("s_waitcnt lgkmcnt(0)" ::: "memory"); SBAR(); } while (0)

constexpr int G1_TILES_B = 16 * 32;
template <int MODE, int NKT>
__device__ __forceinline__ void gemm_tile(const bf16* __restrict__ X, int ldx, const bf16* __restrict__ W, const bf16* __restrict__ W2, int ldw,
                                          void* __restrict__ Cv, bf16* __restrict__ C2, int ldc, int m0, int n0, char* lds) {
    const int tid = threadIdx.x, wid = tid >> 6, lane = tid & 63, r32 = lane & 31, hi = lane >> 5, wm = wid >> 1, wn = wid & 1;
    const unsigned xoff = (unsigned)(((wid * 16 + (lane >> 2)) * ldx + (((lane & 3) ^ ((lane >> 4) & 3)) * 8)) * 2);
    const int bk = ((lane >> 4) & 1) * 8 + (wid >> 1) * 4 + ((lane >> 2) & 3), bc = ((wid & 1) * 2 + (lane >> 5)) * 32 + (lane & 3) * 8;
    const char* Xb = (const char*)(X + (size_t)m0 * ldx);
    const char* Wb; unsigned woff;
    if (MODE != 1) { Wb = (const char*)(W + n0); woff = (unsigned)((bk * ldw + bc) * 2); }
    else { Wb = (const char*)((bc >= 16 && bc < 32) ? W2 : W); woff = (unsigned)((bk * 16 + (bc < 32 ? (bc & 15) : 0)) * 2); }
#define STAGE(bufi, kt) do { _Pragma("unroll") for (int i = 0; i < 2; ++i) \
            __builtin_amdgcn_global_load_lds((const unsigned*)(Xb + (size_t)(64 * i) * ldx * 2 + (kt) * 64 + xoff), (unsigned*)(lds + (bufi) * 16384 + i * 4096 + wid * 1024), 16, 0, 0); \
        _Pragma("unroll") for (int i = 0; i < 2; ++i) \
            __builtin_amdgcn_global_load_lds((const unsigned*)(Wb + (size_t)((kt) * 32 + 16 * i) * ldw * 2 + woff), (unsigned*)(lds + (bufi) * 16384 + 8192 + i * 4096 + wid * 1024), 16, 0, 0); } while (0)
    f32x16 acc[2][2] = {};
    const int arow0 = wm * 64 + r32, asw = (arow0 >> 2) & 3;
    const int vb = (int)(uintptr_t)lds + 8192 + v_rd_base(lane) + wn * 1024;
#define RD2(bufi, ks) \
        bf16x8 a0_##ks = *(const bf16x8*)(lds + (bufi) * 16384 + arow0 * 64 + (((ks * 2 + hi) ^ asw) << 4)); \
        bf16x8 a1_##ks = *(const bf16x8*)(lds + (bufi) * 16384 + (arow0 + 32) * 64 + (((ks * 2 + hi) ^ asw) << 4)); \
        s16x4 l0_##ks, h0_##ks, l1_##ks, h1_##ks; \
        TRRD(l0_##ks, vb, (bufi) * 16384 + ks * 4096); TRRD(h0_##ks, vb, (bufi) * 16384 + ks * 4096 + 2048); \
        TRRD(l1_##ks, vb, (bufi) * 16384 + ks * 4096 + 512); TRRD(h1_##ks, vb, (bufi) * 16384 + ks * 4096 + 2560)
#define MM(ks) do { const bf16x8 b0 = MK8(l0_##ks, h0_##ks), b1 = MK8(l1_##ks, h1_##ks); \
        acc[0][0] = __builtin_amdgcn_mfma_f32_32x32x16_bf16(b0, a0_##ks, acc[0][0], 0, 0, 0); \
        acc[0][1] = __builtin_amdgcn_mfma_f32_32x32x16_bf16(b1, a0_##ks, acc[0][1], 0, 0, 0); \
        acc[1][0] = __builtin_amdgcn_mfma_f32_32x32x16_bf16(b0, a1_##ks, acc[1][0], 0, 0, 0); \
        acc[1][1] = __builtin_amdgcn_mfma_f32_32x32x16_bf16(b1, a1_##ks, acc[1][1], 0, 0, 0); } while (0)
#define STEP(bufi, t, ISSUE, WN) do { asm volatile("s_waitcnt vmcnt(" #WN ")" ::: "memory"); __builtin_amdgcn_s_barrier(); SBAR(); \
        if (ISSUE) STAGE(((bufi) + 3) & 3, (t) + 3); \
        RD2(bufi, 0); RD2(bufi, 1); LGKM0(); MM(0); MM(1); SBAR(); } while (0)
    constexpr int NS = NKT * 2;
    STAGE(0, 0); STAGE(1, 1); STAGE(2, 2);
#pragma unroll 1
    for (int t = 0; t < NS - 4; t += 4) { STEP(0, t, true, 8); STEP(1, t + 1, true, 8); STEP(2, t + 2, true, 8); STEP(3, t + 3, true, 8); }
    STEP(0, NS - 4, true, 8); STEP(1, NS - 3, false, 8); STEP(2, NS - 2, false, 4); STEP(3, NS - 1, false, 0);
    __syncthreads();
    if (MODE == 2) {
        float* C = (float*)Cv;
#pragma unroll
        for (int mt = 0; mt < 2; ++mt)
#pragma unroll
            for (int nt = 0; nt < 2; ++nt)
#pragma unroll
                for (int g = 0; g < 4; ++g) { const int m = wm * 64 + mt * 32 + r32, n = wn * 64 + nt * 32 + 8 * g + 4 * hi;
                    f32x4 w = {acc[mt][nt][4 * g], acc[mt][nt][4 * g + 1], acc[mt][nt][4 * g + 2], acc[mt][nt][4 * g + 3]};
                    *(f32x4*)(C + (size_t)(m0 + m) * ldc + n0 + n) = w; }
        return;
    }
    bf16* C = (bf16*)Cv;
    char* Cs = lds;
#pragma unroll
    for (int mt = 0; mt < 2; ++mt)
#pragma unroll
        for (int nt = 0; nt < 2; ++nt)
#pragma unroll
            for (int g = 0; g < 4; ++g) { const int m = wm * 64 + mt * 32 + r32, n = wn * 64 + nt * 32 + 8 * g + 4 * hi;
                u32x2 w = {cvtpk(acc[mt][nt][4 * g], acc[mt][nt][4 * g + 1]), cvtpk(acc[mt][nt][4 * g + 2], acc[mt][nt][4 * g + 3])};
                *(u32x2*)(Cs + m * 272 + n * 2) = w; }
    __syncthreads();
    if (MODE == 0) {
#pragma unroll
        for (int i = 0; i < 8; ++i) { const int c = tid + 256 * i, row = c >> 4, ch = c & 15;
            *(bf16x8*)(C + (size_t)(m0 + row) * ldc + n0 + ch * 8) = *(const bf16x8*)(Cs + row * 272 + ch * 16); }
    } else {
        { const int row = tid >> 1, ch = tid & 1; *(bf16x8*)(C + (size_t)(m0 + row) * 16 + ch * 8) = *(const bf16x8*)(Cs + row * 272 + ch * 16); }
#pragma unroll
        for (int i = 0; i < 8; ++i) { const int c = tid + 256 * i, d = c >> 7, row = c & 127;
            C2[(size_t)d * S + m0 + row] = *(const bf16*)(Cs + row * 272 + 32 + d * 2); }
    }
#undef STAGE
#undef RD2
#undef MM
#undef STEP
}
__global__ __launch_bounds__(256, 2)
void proj_kernel(const bf16* __restrict__ Ax, const bf16* __restrict__ Awq, const bf16* __restrict__ Awk, const bf16* __restrict__ Awv,
                 const bf16* __restrict__ Bx, const bf16* __restrict__ Bc, const bf16* __restrict__ Bwq, const bf16* __restrict__ Bwuk,
                 const bf16* __restrict__ Bwuv, char* __restrict__ ws) {
    extern __shared__ __attribute__((aligned(16))) char lds[];
    int id = blockIdx.x;
    if (id >= 3 * G1_TILES_B + 64) { const int t = id - 3 * G1_TILES_B - 64;
        gemm_tile<1, 8>(Ax, MD, Awk, Awv, 16, ws + WS_KA, (bf16*)(ws + WS_VAT), 16, t * 128, 0, lds); return; }
    if (id < 3 * G1_TILES_B) id = (id & 7) * (3 * G1_TILES_B / 8) + (id >> 3);
    const bf16* X; const bf16* W; bf16* C; int ld, t;
    if (id < 3 * G1_TILES_B) { const int g = id / G1_TILES_B; t = id - g * G1_TILES_B;
        X = g == 0 ? Bx : Bc; W = g == 0 ? Bwq : (g == 1 ? Bwuk : Bwuv); C = (bf16*)(ws + (g == 0 ? WS_QB : (g == 1 ? WS_KB : WS_VB))); ld = HD; }
    else { t = id - 3 * G1_TILES_B; X = Ax; W = Awq; C = (bf16*)(ws + WS_QA); ld = MD; }
    gemm_tile<0, 8>(X, MD, W, W, ld, C, C, ld, (t & 15) * 128, (t >> 4) * 128, lds);
}

constexpr float SCALE_B = 0.08838834764831845f, SCALE_A = 0.25f, THR = 8.f, LOG2E = 1.4426950408889634f;
constexpr int NW = 4, QBLK = 32, KVBLK = 64, QB = NW * QBLK;
constexpr int SHM_V = KVBLK * DB * 2, SHM_K = KVBLK * DB * 2;
constexpr int LDS_ATT = 2 * SHM_V + 2 * SHM_K + NW * 64 * 4;
#define KSWZ(row, colB) ((row) * 256 + ((colB) ^ (((row) & 7) << 4)))

__device__ __forceinline__ void mask_tile(f32x16& p0, f32x16& p1, int dq) {
    const float NEG = -__builtin_inff();
#pragma unroll
    for (int r = 0; r < 16; ++r) { const int c = (r & 3) + 8 * (r >> 2);
        if (dq - c < 0) p0[r] = NEG;
        if (dq - c - 32 < 0) p1[r] = NEG; }
}
__device__ __forceinline__ void partialSM(f32x16& p0, f32x16& p1, float& m_reg, float& mn, float& alpha) {
    float pmax = p0[0]; for (int r = 1; r < 16; ++r) pmax = fmaxf(pmax, p0[r]); for (int r = 0; r < 16; ++r) pmax = fmaxf(pmax, p1[r]);
    { auto rr = __builtin_amdgcn_permlane32_swap(__float_as_uint(pmax), __float_as_uint(pmax), false, false);
      pmax = fmaxf(__uint_as_float(rr[0]), __uint_as_float(rr[1])); }
    constexpr float C2 = LOG2E * SCALE_B;
    if (__builtin_expect(__all((pmax - m_reg) * SCALE_B <= THR), 1)) { mn = m_reg; alpha = 1.f; }
    else { mn = fmaxf(m_reg, pmax); alpha = __builtin_amdgcn_exp2f((m_reg - mn) * C2); m_reg = mn; }
    const float mnL = -mn * C2;
    for (int r = 0; r < 16; ++r) p0[r] = fmaf(p0[r], C2, mnL); for (int r = 0; r < 16; ++r) p1[r] = fmaf(p1[r], C2, mnL);
    for (int r = 0; r < 16; ++r) p0[r] = __builtin_amdgcn_exp2f(p0[r]);
}
__device__ __forceinline__ void finishSM(f32x16& p0, f32x16& p1, float alpha, float& l_reg, bf16x8& pa0, bf16x8& pa1, bf16x8& pa2, bf16x8& pa3) {
    for (int r = 0; r < 16; ++r) p1[r] = __builtin_amdgcn_exp2f(p1[r]);
    float ps = 0; for (int r = 0; r < 16; ++r) ps += p0[r]; for (int r = 0; r < 16; ++r) ps += p1[r];
    { auto rr = __builtin_amdgcn_permlane32_swap(__float_as_uint(ps), __float_as_uint(ps), false, false);
      ps = __uint_as_float(rr[0]) + __uint_as_float(rr[1]); }
    l_reg = l_reg * alpha + ps;
#define PK4(P, B_, OUT) do { unsigned a0 = cvtpk(P[B_+0], P[B_+1]), a1 = cvtpk(P[B_+2], P[B_+3]);                          \
        unsigned b0 = cvtpk(P[B_+4], P[B_+5]), b1 = cvtpk(P[B_+6], P[B_+7]);                                             \
        auto r0 = __builtin_amdgcn_permlane32_swap(a0, b0, false, false); auto r1 = __builtin_amdgcn_permlane32_swap(a1, b1, false, false); \
        u32x4 w = {r0[0], r1[0], r0[1], r1[1]}; OUT = *reinterpret_cast<bf16x8*>(&w); } while (0)
    PK4(p0, 0, pa0); PK4(p0, 8, pa1); PK4(p1, 0, pa2); PK4(p1, 8, pa3);
#undef PK4
}
template <int KB>
__device__ __forceinline__ void qkt(f32x16& p0, f32x16& p1, const char* K_lds, int r32, int hi, const bf16x8* qr) {
    p0 = f32x16{}; p1 = f32x16{};
    const char* kb[4];
#pragma unroll
    for (int dd = 0; dd < 4; ++dd) kb[dd] = K_lds + KB * SHM_K + KSWZ(r32, (dd * 16 + hi * 8) * 2);
#pragma unroll
    for (int d0 = 0; d0 < 8; ++d0) { const char* a = kb[d0 & 3] + (d0 >> 2) * 128;
        bf16x8 b0 = *reinterpret_cast<const bf16x8*>(a);
        bf16x8 b1 = *reinterpret_cast<const bf16x8*>(a + 32 * 256);
        p0 = __builtin_amdgcn_mfma_f32_32x32x16_bf16(b0, qr[d0], p0, 0, 0, 0);
        p1 = __builtin_amdgcn_mfma_f32_32x32x16_bf16(b1, qr[d0], p1, 0, 0, 0); }
}
template <int VB>
__device__ __forceinline__ void pv_tile(f32x16* o, int vb0, bf16x8 pa0, bf16x8 pa1, bf16x8 pa2, bf16x8 pa3) {
#define PV_RD(S_, ks) do { constexpr int b_ = VB * SHM_V + (ks) * 4096;   \
        TRRD(S_##l0, vb0, b_); TRRD(S_##h0, vb0, b_ + 2048); TRRD(S_##l1, vb0, b_ + 512); TRRD(S_##h1, vb0, b_ + 2560); \
        TRRD(S_##l2, vb0, b_ + 1024); TRRD(S_##h2, vb0, b_ + 3072); TRRD(S_##l3, vb0, b_ + 1536); TRRD(S_##h3, vb0, b_ + 3584); } while (0)
#define PV_MM(S_, pa) do {   \
        o[0] = __builtin_amdgcn_mfma_f32_32x32x16_bf16(pa, MK8(S_##l0, S_##h0), o[0], 0, 0, 0);   \
        o[1] = __builtin_amdgcn_mfma_f32_32x32x16_bf16(pa, MK8(S_##l1, S_##h1), o[1], 0, 0, 0);   \
        o[2] = __builtin_amdgcn_mfma_f32_32x32x16_bf16(pa, MK8(S_##l2, S_##h2), o[2], 0, 0, 0);   \
        o[3] = __builtin_amdgcn_mfma_f32_32x32x16_bf16(pa, MK8(S_##l3, S_##h3), o[3], 0, 0, 0); } while (0)
    s16x4 xl0, xl1, xl2, xl3, xh0, xh1, xh2, xh3, yl0, yl1, yl2, yl3, yh0, yh1, yh2, yh3;
    PV_RD(x, 0); LGKM0();
    PV_RD(y, 1); SBAR(); PV_MM(x, pa0); LGKM0();
    PV_RD(x, 2); SBAR(); PV_MM(y, pa1); LGKM0();
    PV_RD(y, 3); SBAR(); PV_MM(x, pa2); LGKM0();
    PV_MM(y, pa3);
#undef PV_RD
#undef PV_MM
}
struct Seam { bf16x8 qr[8]; bf16x8 sv[4]; };
__device__ __forceinline__ void k_dma(const bf16* Kh, int k0, char* K_lds, int bufi, int wid, unsigned koff) {
#pragma unroll
    for (int i = 0; i < 4; ++i) { const char* ub = (const char*)Kh + (size_t)(k0 + 16 * i) * (HD * 2);
        __builtin_amdgcn_global_load_lds((const unsigned*)(ub + koff), (unsigned*)(K_lds + bufi * SHM_K + i * 4096 + wid * 1024), 16, 0, 0); }
}
#define VLOAD(Vp, k0) do { _Pragma("unroll") for (int i_ = 0; i_ < 4; ++i_) { const char* ub_ = (const char*)(Vp) + (size_t)((k0) + 16 * i_) * (HD * 2); Sm.sv[i_] = *(const bf16x8*)(ub_ + voff); } } while (0)
#define VWRITE(bf) do { _Pragma("unroll") for (int i_ = 0; i_ < 4; ++i_) *(bf16x8*)(V_lds + (bf) * SHM_V + vst + i_ * 4096) = Sm.sv[i_]; } while (0)

__device__ __forceinline__ void attnB_block(const bf16* Qh, const bf16* Kh, const bf16* Vh, bf16* Oh, int P0, int P0n, char* lds, Seam& Sm) {
    const int tid = threadIdx.x, wid = __builtin_amdgcn_readfirstlane(tid >> 6), lane = tid & 63, r32 = lane & 31, hi = lane >> 5;
    const int NT = (P0 + QB) / KVBLK;
    const int qlo = P0 + wid * QBLK, qm = qlo + r32 - 4 * hi;
    char* V_lds = lds; char* K_lds = lds + 2 * SHM_V;
    float* wsf = (float*)(lds + 2 * SHM_V + 2 * SHM_K) + wid * 64; float* li_l = wsf, * al_l = wsf + 32;
    float m_reg = -1e30f, l_reg = 0; f32x16 o[4] = {};
    const int sr = tid >> 4, sc = (tid & 15) * 8, vst = v_st<4>(sr, sc);
    const int vb0 = (int)(uintptr_t)V_lds + v_rd_base(lane);
    const unsigned krow = wid * 4 + (lane >> 4), koff = (krow * HD + (((lane & 15) ^ (krow & 7)) * 8)) * 2, voff = (sr * HD + sc) * 2;
#define RESC(a) do { if (__any((a) < 1.f)) { if (hi == 0) al_l[r32] = (a); asm volatile("s_waitcnt lgkmcnt(0)" ::: "memory");              \
                     for (int d_ = 0; d_ < 4; ++d_) for (int r = 0; r < 16; ++r) o[d_][r] *= al_l[crow(r, hi)]; } } while (0)
#define MASKT(P0_, P1_, t) do { const int kb_ = (t) * KVBLK; if (kb_ + KVBLK - 1 > qlo) mask_tile(P0_, P1_, qm - kb_); } while (0)
    f32x16 pA0, pA1, pB0, pB1; float mnA, mnB, alA, alB; bf16x8 pa0, pa1, pa2, pa3;
    VWRITE(0); SBAR();
    VLOAD(Vh, KVBLK); k_dma(Kh, KVBLK, K_lds, 1, wid, koff);
    SBAR(); qkt<0>(pA0, pA1, K_lds, r32, hi, Sm.qr);
    MASKT(pA0, pA1, 0); partialSM(pA0, pA1, m_reg, mnA, alA);
    VMW(); VWRITE(1);
    __syncthreads();
#define HALF_STEP(PX0, PX1, mnX, alX, PY0, PY1, alY, t, KB, VB, SB) do {                                                      \
        SBAR(); qkt<KB>(PX0, PX1, K_lds, r32, hi, Sm.qr);                                                                     \
        finishSM(PY0, PY1, alY, l_reg, pa0, pa1, pa2, pa3); SBAR();                                                           \
        VLOAD(Vh, ((t) + 1) * KVBLK); k_dma(Kh, ((t) + 1) * KVBLK, K_lds, SB, wid, koff); SBAR();                             \
        pv_tile<VB>(o, vb0, pa0, pa1, pa2, pa3); MASKT(PX0, PX1, (t)); partialSM(PX0, PX1, m_reg, mnX, alX);                  \
        __syncthreads();                                                                                                      \
        VMW(); VWRITE(SB);                                                                                                    \
        RESC(alX); __syncthreads(); } while (0)
    for (int t = 1; t + 1 < NT; t += 2) {
        HALF_STEP(pB0, pB1, mnB, alB, pA0, pA1, alA, t, 1, 0, 0);
        HALF_STEP(pA0, pA1, mnA, alA, pB0, pB1, alB, t + 1, 0, 1, 1);
    }
    SBAR(); qkt<1>(pB0, pB1, K_lds, r32, hi, Sm.qr); SBAR();
    VLOAD(Vh, 0); SBAR();
    {   int lz = lane; asm volatile("" : "+v"(lz));
        const unsigned qoff = ((lz & 31) * HD + (lz >> 5) * 8) * 2; const char* ub = (const char*)Qh + (size_t)(P0n + wid * QBLK) * (HD * 2);
#pragma unroll
        for (int d0 = 0; d0 < 8; ++d0) Sm.qr[d0] = *(const bf16x8*)(ub + qoff + d0 * 32); }
    SBAR();
    finishSM(pA0, pA1, alA, l_reg, pa0, pa1, pa2, pa3); SBAR();
    pv_tile<0>(o, vb0, pa0, pa1, pa2, pa3);
    MASKT(pB0, pB1, NT - 1); partialSM(pB0, pB1, m_reg, mnB, alB); __syncthreads();
    k_dma(Kh, 0, K_lds, 0, wid, koff);
    RESC(alB);
    finishSM(pB0, pB1, alB, l_reg, pa0, pa1, pa2, pa3); SBAR(); pv_tile<1>(o, vb0, pa0, pa1, pa2, pa3);
    SBAR();
    if (hi == 0) li_l[r32] = l_reg; asm volatile("s_waitcnt lgkmcnt(0)" ::: "memory");
    float rli[16];
#pragma unroll
    for (int r = 0; r < 16; ++r) rli[r] = __builtin_amdgcn_rcpf(li_l[crow(r, hi)]);
    char* Ow = (char*)(Oh + (size_t)(P0 + wid * QBLK) * HD);
    {   int lz = lane; asm volatile("" : "+v"(lz));
        const unsigned ooff = ((lz >> 5) * 4 * HD + (lz & 31)) * 2;
#pragma unroll
        for (int r = 0; r < 16; ++r) { const int orow = (r & 3) + 8 * (r >> 2);
#pragma unroll
            for (int d0 = 0; d0 < 4; ++d0) { const float v = o[d0][r] * rli[r]; const float vn = __shfl_xor(v, 1);
                if ((lz & 1) == 0) *(unsigned*)(Ow + (size_t)(orow * HD * 2 + d0 * 64) + ooff) = cvtpk(v, vn); } } }
    VMW();
    __syncthreads();
#undef RESC
#undef MASKT
#undef HALF_STEP
}

__device__ __forceinline__ void attnA_block(int a, const char* ws) {
    const int tid = threadIdx.x, wid = tid >> 6, lane = tid & 63, r32 = lane & 31, hi = lane >> 5;
    const int h = a >> 3, q0 = (a & 7) * 256 + wid * 64;
    const bf16* QA = (const bf16*)(ws + WS_QA); const bf16* KA = (const bf16*)(ws + WS_KA); const bf16* VAT = (const bf16*)(ws + WS_VAT);
    bf16* CA = (bf16*)(ws + WS_CA);
    bf16x8 qf[2];
#pragma unroll
    for (int s = 0; s < 2; ++s) qf[s] = *(const bf16x8*)(QA + (size_t)(q0 + s * 32 + r32) * MD + h * 16 + hi * 8);
    f32x16 o[2] = {}; float m_reg[2] = {-1e30f, -1e30f};
    constexpr float C2 = LOG2E * SCALE_A;
    const bf16* kp = KA + (size_t)r32 * 16 + hi * 8;
    const bf16* vp = VAT + (size_t)(r32 & 15) * S + 4 * hi;
    const bool vrow = r32 < 16;
    const short one = (r32 == 16) ? (short)0x3F80 : (short)0;
    bf16x8 kf = *(const bf16x8*)(kp);
    s16x4 v00 = *(const s16x4*)(vp), v01 = *(const s16x4*)(vp + 8), v10 = *(const s16x4*)(vp + 16), v11 = *(const s16x4*)(vp + 24);
#pragma unroll 1
    for (int k0 = 0; k0 < S; k0 += 32) {
        const bf16x8 kc = kf; bf16x8 va0, va1;
        { const s16x4 o4 = {one, one, one, one};
          const s16x4 x00 = vrow ? v00 : o4, x01 = vrow ? v01 : o4, x10 = vrow ? v10 : o4, x11 = vrow ? v11 : o4;
          va0 = MK8(x00, x01); va1 = MK8(x10, x11); }
        const int kn = (k0 + 32 < S) ? k0 + 32 : k0;
        kf = *(const bf16x8*)(kp + (size_t)kn * 16);
        v00 = *(const s16x4*)(vp + kn); v01 = *(const s16x4*)(vp + kn + 8); v10 = *(const s16x4*)(vp + kn + 16); v11 = *(const s16x4*)(vp + kn + 24);
        f32x16 p[2];
#pragma unroll
        for (int s = 0; s < 2; ++s) p[s] = __builtin_amdgcn_mfma_f32_32x32x16_bf16(kc, qf[s], f32x16{}, 0, 0, 0);
#pragma unroll
        for (int s = 0; s < 2; ++s) {
            float pmax = p[s][0];
#pragma unroll
            for (int r = 1; r < 16; ++r) pmax = fmaxf(pmax, p[s][r]);
            { auto rr = __builtin_amdgcn_permlane32_swap(__float_as_uint(pmax), __float_as_uint(pmax), false, false);
              pmax = fmaxf(__uint_as_float(rr[0]), __uint_as_float(rr[1])); }
            if (!__builtin_expect(__all((pmax - m_reg[s]) * SCALE_A <= THR), 1)) {
                const float mn = fmaxf(m_reg[s], pmax), alpha = __builtin_amdgcn_exp2f((m_reg[s] - mn) * C2); m_reg[s] = mn;
#pragma unroll
                for (int r = 0; r < 9; ++r) o[s][r] *= alpha;
            }
            const float mnL = -m_reg[s] * C2;
#pragma unroll
            for (int r = 0; r < 16; ++r) p[s][r] = __builtin_amdgcn_exp2f(fmaf(p[s][r], C2, mnL));
            u32x4 w0 = {cvtpk(p[s][0], p[s][1]), cvtpk(p[s][2], p[s][3]), cvtpk(p[s][4], p[s][5]), cvtpk(p[s][6], p[s][7])};
            u32x4 w1 = {cvtpk(p[s][8], p[s][9]), cvtpk(p[s][10], p[s][11]), cvtpk(p[s][12], p[s][13]), cvtpk(p[s][14], p[s][15])};
            o[s] = __builtin_amdgcn_mfma_f32_32x32x16_bf16(va0, *reinterpret_cast<bf16x8*>(&w0), o[s], 0, 0, 0);
            o[s] = __builtin_amdgcn_mfma_f32_32x32x16_bf16(va1, *reinterpret_cast<bf16x8*>(&w1), o[s], 0, 0, 0);
        }
    }
#pragma unroll
    for (int s = 0; s < 2; ++s) {
        float l = o[s][8];
        { auto rr = __builtin_amdgcn_permlane32_swap(__float_as_uint(l), __float_as_uint(l), false, false); l = __uint_as_float(rr[0]); }
        const float rl = 1.f / l;
        bf16* op = CA + (size_t)(q0 + s * 32 + r32) * MD + h * 16 + 4 * hi;
        u32x2 w0 = {cvtpk(o[s][0] * rl, o[s][1] * rl), cvtpk(o[s][2] * rl, o[s][3] * rl)};
        u32x2 w1 = {cvtpk(o[s][4] * rl, o[s][5] * rl), cvtpk(o[s][6] * rl, o[s][7] * rl)};
        *(u32x2*)op = w0; *(u32x2*)(op + 8) = w1;
    }
}

__global__ __launch_bounds__(256, 2)
void attn_kernel(char* __restrict__ ws) {
    extern __shared__ __attribute__((aligned(16))) char lds[];
    const int id = blockIdx.x;
    if (id >= 256) { attnA_block(id - 256, ws); return; }
    const int h = (id & 7) * 4 + (id >> 6), x = (id >> 3) & 7;
    const bf16* Qh = (const bf16*)(ws + WS_QB) + h * DB; const bf16* Kh = (const bf16*)(ws + WS_KB) + h * DB;
    const bf16* Vh = (const bf16*)(ws + WS_VB) + h * DB; bf16* Oh = (bf16*)(ws + WS_CB) + h * DB;
    const int Pa = (15 - x) * QB, Pb = x * QB;
    Seam Sm;
    {   const int tid = threadIdx.x, wid = __builtin_amdgcn_readfirstlane(tid >> 6), lane = tid & 63, r32 = lane & 31, hi = lane >> 5;
        const int sr = tid >> 4, sc = (tid & 15) * 8;
        const unsigned krow = wid * 4 + (lane >> 4), koff = (krow * HD + (((lane & 15) ^ (krow & 7)) * 8)) * 2, voff = (sr * HD + sc) * 2;
#pragma unroll
        for (int d0 = 0; d0 < 8; ++d0) Sm.qr[d0] = *(const bf16x8*)(Qh + (size_t)(Pa + wid * QBLK + r32) * HD + d0 * 16 + hi * 8);
        VLOAD(Vh, 0); k_dma(Kh, 0, lds + 2 * SHM_V, 0, wid, koff); VMW(); __syncthreads(); }
#pragma unroll 1
    for (int pass = 0; pass < 2; ++pass) attnB_block(Qh, Kh, Vh, Oh, pass ? Pb : Pa, Pb, lds, Sm);
}

__global__ __launch_bounds__(256, 2)
void outproj_kernel(char* __restrict__ ws, const bf16* __restrict__ Awo, const bf16* __restrict__ Bwo, bf16* __restrict__ out) {
    extern __shared__ __attribute__((aligned(16))) char lds[];
    int id = blockIdx.x;
    if (id < 256) { id = (id & 7) * 32 + (id >> 3);
        const int tm = id >> 4, sp = (id >> 2) & 3, tn = id & 3;
        gemm_tile<2, 16>((const bf16*)(ws + WS_CB) + sp * 1024, HD, Bwo + (size_t)sp * 1024 * MD, Bwo, MD,
                         (float*)(ws + WS_PART) + (size_t)sp * S * MD, nullptr, MD, tm * 128, tn * 128, lds);
    } else { const int t = id - 256;
        gemm_tile<0, 8>((const bf16*)(ws + WS_CA), MD, Awo, Awo, MD, out, nullptr, 1024, (t >> 2) * 128, (t & 3) * 128, lds); }
}
__global__ __launch_bounds__(256)
void reduce_kernel(const char* __restrict__ ws, bf16* __restrict__ out) {
    const int c = blockIdx.x * 256 + threadIdx.x, row = c >> 6, col = (c & 63) * 8;
    const float* P = (const float*)(ws + WS_PART) + (size_t)row * MD + col;
    f32x4 s0 = {}, s1 = {};
#pragma unroll
    for (int sp = 0; sp < 4; ++sp) { s0 += *(const f32x4*)(P + (size_t)sp * S * MD); s1 += *(const f32x4*)(P + (size_t)sp * S * MD + 4); }
    u32x4 o = {cvtpk(s0[0], s0[1]), cvtpk(s0[2], s0[3]), cvtpk(s1[0], s1[1]), cvtpk(s1[2], s1[3])};
    *(u32x4*)(out + (size_t)row * 1024 + 512 + col) = o;
}

extern "C" void kernel_launch(void* const* d_in, const int* in_sizes, int n_in, void* d_out, int out_size, void* d_ws, size_t ws_size,
                              hipStream_t stream) {
    (void)in_sizes; (void)n_in; (void)out_size; (void)ws_size;
    (void)hipFuncSetAttribute((const void*)attn_kernel, hipFuncAttributeMaxDynamicSharedMemorySize, LDS_ATT);
    (void)hipFuncSetAttribute((const void*)outproj_kernel, hipFuncAttributeMaxDynamicSharedMemorySize, 65536);
    (void)hipFuncSetAttribute((const void*)proj_kernel, hipFuncAttributeMaxDynamicSharedMemorySize, 65536);
    char* ws = (char*)d_ws;
    proj_kernel<<<3 * G1_TILES_B + 64 + 16, 256, 65536, stream>>>((const bf16*)d_in[0], (const bf16*)d_in[1], (const bf16*)d_in[2], (const bf16*)d_in[3],
        (const bf16*)d_in[5], (const bf16*)d_in[6], (const bf16*)d_in[7], (const bf16*)d_in[8], (const bf16*)d_in[9], ws);
    attn_kernel<<<512, 256, LDS_ATT, stream>>>(ws);
    outproj_kernel<<<256 + 64, 256, 65536, stream>>>(ws, (const bf16*)d_in[4], (const bf16*)d_in[10], (bf16*)d_out);
    reduce_kernel<<<512, 256, 0, stream>>>(ws, (bf16*)d_out);
}
```

```cpp
#include <hip/hip_runtime.h>
#include <hip/hip_bf16.h>
#include <cstdint>

using bf16 = __hip_bfloat16;
typedef short bf16x8 __attribute__((ext_vector_type(8)));
typedef short s16x4 __attribute__((ext_vector_type(4)));
typedef float f32x16 __attribute__((ext_vector_type(16)));
typedef float f32x4 __attribute__((ext_vector_type(4)));
typedef unsigned u32x4 __attribute__((ext_vector_type(4)));
typedef unsigned u32x2 __attribute__((ext_vector_type(2)));

constexpr int S = 2048, MD = 512, NH = 32, DB = 128, HD = NH * DB;
constexpr size_t WS_QB = 0, WS_KB = WS_QB + (size_t)S * HD * 2, WS_VB = WS_KB + (size_t)S * HD * 2, WS_CB = WS_QB;
constexpr size_t WS_QA = WS_VB + (size_t)S * HD * 2, WS_CA = WS_QA, WS_KA = WS_QA + (size_t)S * MD * 2, WS_VAT = WS_KA + (size_t)S * 16 * 2, WS_PART = WS_KB;

#define SBAR() __builtin_amdgcn_sched_barrier(0)
#define VMW() asm volatile("s_waitcnt vmcnt(0)" ::: "memory")
__device__ __forceinline__ int crow(int r, int hi) { return (r & 3) + 8 * (r >> 2) + 4 * hi; }
__device__ __forceinline__ unsigned cvtpk(float lo, float hi) {
    unsigned r; asm volatile("v_cvt_pk_bf16_f32 %0, %1, %2" : "=v"(r) : "v"(lo), "v"(hi)); return r;
}
template <int NC> __device__ __forceinline__ int v_st(int k, int c) { const int kk = (k & ~0xC) | ((k & 4) << 1) | ((k & 8) >> 1); return ((kk >> 3) * NC + (c >> 5)) * 512 + ((kk & 7) * 32 + (c & 31)) * 2; }
__device__ __forceinline__ int v_rd_base(int lane) { return ((lane & 3) << 3) | (((lane >> 2) & 3) << 6) | (((lane >> 4) & 1) << 5) | (((lane >> 5) & 1) << 8); }
#define TRRD(dst, base, off) asm volatile("ds_read_b64_tr_b16 %0, %1 offset:%2" : "=&v"(dst) : "v"(base), "i"(off) : "memory")
#define MK8(l, h) (bf16x8){l[0], l[1], l[2], l[3], h[0], h[1], h[2], h[3]}
#define ILV(nv) do { _Pragma("unroll") for (int g_ = 0; g_ < 4; ++g_) { __builtin_amdgcn_sched_group_barrier(0x0008, 1, 0); __builtin_amdgcn_sched_group_barrier(0x0002, nv, 0); } } while (0)
#define LGKM0() do { asm volatile("s_waitcnt lgkmcnt(0)" ::: "memory"); SBAR(); } while (0)

constexpr int G1_TILES_B = 16 * 32;
template <int MODE, int NKT>
__device__ __forceinline__ void gemm_tile(const bf16* __restrict__ X, int ldx, const bf16* __restrict__ W, const bf16* __restrict__ W2, int ldw,
                                          void* __restrict__ Cv, bf16* __restrict__ C2, int ldc, int m0, int n0, char* lds) {
    const int tid = threadIdx.x, wid = tid >> 6, lane = tid & 63, r32 = lane & 31, hi = lane >> 5, wm = wid >> 1, wn = wid & 1;
    const unsigned xoff = (unsigned)(((wid * 8 + (lane >> 3)) * ldx + (((lane & 7) ^ ((wid * 4 + (lane >> 4)) & 7)) * 8)) * 2);
    const int bk = wid * 4 + (lane >> 4), bch = (lane & 15) ^ (((lane >> 4) & 3) << 2);
    const char* Xb = (const char*)(X + (size_t)m0 * ldx);
    const char* Wb; unsigned woff;
    if (MODE != 1) { Wb = (const char*)(W + n0); woff = (unsigned)((bk * ldw + bch * 8) * 2); }
    else { Wb = (const char*)((bch >= 2 && bch < 4) ? W2 : W); woff = (unsigned)((bk * 16 + (bch < 4 ? (bch & 1) * 8 : 0)) * 2); }
#define STAGE(bufi, kt) do { _Pragma("unroll") for (int i = 0; i < 4; ++i) \
            __builtin_amdgcn_global_load_lds((const unsigned*)(Xb + (size_t)(32 * i) * ldx * 2 + (kt) * 128 + xoff), (unsigned*)(lds + (bufi) * 32768 + i * 4096 + wid * 1024), 16, 0, 0); \
        _Pragma("unroll") for (int i = 0; i < 4; ++i) \
            __builtin_amdgcn_global_load_lds((const unsigned*)(Wb + (size_t)((kt) * 64 + 16 * i) * ldw * 2 + woff), (unsigned*)(lds + (bufi) * 32768 + 16384 + i * 4096 + wid * 1024), 16, 0, 0); } while (0)
    f32x16 acc[2][2] = {};
    const int arow0 = wm * 64 + r32, asw = (arow0 >> 1) & 7;
    const int tk = 8 * (lane >> 5) + ((lane >> 2) & 3), tcc = 2 * ((lane >> 4) & 1) + ((lane & 3) >> 1);
    const int vb0 = (int)(uintptr_t)lds + 16384 + tk * 256 + ((4 * ((2 * wn) ^ ((lane >> 2) & 3)) + tcc) << 4) + (lane & 1) * 8;
    const int vb1 = (int)(uintptr_t)lds + 16384 + tk * 256 + ((4 * ((2 * wn + 1) ^ ((lane >> 2) & 3)) + tcc) << 4) + (lane & 1) * 8;
#define RD2(bufi, ks) \
        bf16x8 a0_##ks = *(const bf16x8*)(lds + (bufi) * 32768 + arow0 * 128 + (((ks * 2 + hi) ^ asw) << 4)); \
        bf16x8 a1_##ks = *(const bf16x8*)(lds + (bufi) * 32768 + (arow0 + 32) * 128 + (((ks * 2 + hi) ^ asw) << 4)); \
        s16x4 l0_##ks, h0_##ks, l1_##ks, h1_##ks; \
        TRRD(l0_##ks, vb0, (bufi) * 32768 + ks * 4096); TRRD(h0_##ks, vb0, (bufi) * 32768 + ks * 4096 + 1024); \
        TRRD(l1_##ks, vb1, (bufi) * 32768 + ks * 4096); TRRD(h1_##ks, vb1, (bufi) * 32768 + ks * 4096 + 1024)
#define MM(ks) do { const bf16x8 b0 = MK8(l0_##ks, h0_##ks), b1 = MK8(l1_##ks, h1_##ks); \
        acc[0][0] = __builtin_amdgcn_mfma_f32_32x32x16_bf16(b0, a0_##ks, acc[0][0], 0, 0, 0); \
        acc[0][1] = __builtin_amdgcn_mfma_f32_32x32x16_bf16(b1, a0_##ks, acc[0][1], 0, 0, 0); \
        acc[1][0] = __builtin_amdgcn_mfma_f32_32x32x16_bf16(b0, a1_##ks, acc[1][0], 0, 0, 0); \
        acc[1][1] = __builtin_amdgcn_mfma_f32_32x32x16_bf16(b1, a1_##ks, acc[1][1], 0, 0, 0); } while (0)
#define STEP(bufi, t, ISSUE) do { VMW(); __builtin_amdgcn_s_barrier(); SBAR(); \
        if (ISSUE) STAGE((bufi) ^ 1, (t) + 1); \
        RD2(bufi, 0); RD2(bufi, 1); LGKM0(); RD2(bufi, 2); RD2(bufi, 3); SBAR(); MM(0); MM(1); SBAR(); LGKM0(); MM(2); MM(3); SBAR(); } while (0)
    STAGE(0, 0);
#pragma unroll 1
    for (int t = 0; t < NKT - 2; t += 2) { STEP(0, t, true); STEP(1, t + 1, true); }
    STEP(0, NKT - 2, true); STEP(1, NKT - 1, false);
    __syncthreads();
    if (MODE == 2) {
        float* C = (float*)Cv;
#pragma unroll
        for (int mt = 0; mt < 2; ++mt)
#pragma unroll
            for (int nt = 0; nt < 2; ++nt)
#pragma unroll
                for (int g = 0; g < 4; ++g) { const int m = wm * 64 + mt * 32 + r32, n = wn * 64 + nt * 32 + 8 * g + 4 * hi;
                    f32x4 w = {acc[mt][nt][4 * g], acc[mt][nt][4 * g + 1], acc[mt][nt][4 * g + 2], acc[mt][nt][4 * g + 3]};
                    *(f32x4*)(C + (size_t)(m0 + m) * ldc + n0 + n) = w; }
        return;
    }
    bf16* C = (bf16*)Cv;
    char* Cs = lds;
#pragma unroll
    for (int mt = 0; mt < 2; ++mt)
#pragma unroll
        for (int nt = 0; nt < 2; ++nt)
#pragma unroll
            for (int g = 0; g < 4; ++g) { const int m = wm * 64 + mt * 32 + r32, n = wn * 64 + nt * 32 + 8 * g + 4 * hi;
                u32x2 w = {cvtpk(acc[mt][nt][4 * g], acc[mt][nt][4 * g + 1]), cvtpk(acc[mt][nt][4 * g + 2], acc[mt][nt][4 * g + 3])};
                *(u32x2*)(Cs + m * 272 + n * 2) = w; }
    __syncthreads();
    if (MODE == 0) {
#pragma unroll
        for (int i = 0; i < 8; ++i) { const int c = tid + 256 * i, row = c >> 4, ch = c & 15;
            *(bf16x8*)(C + (size_t)(m0 + row) * ldc + n0 + ch * 8) = *(const bf16x8*)(Cs + row * 272 + ch * 16); }
    } else {
        { const int row = tid >> 1, ch = tid & 1; *(bf16x8*)(C + (size_t)(m0 + row) * 16 + ch * 8) = *(const bf16x8*)(Cs + row * 272 + ch * 16); }
#pragma unroll
        for (int i = 0; i < 8; ++i) { const int c = tid + 256 * i, d = c >> 7, row = c & 127;
            C2[(size_t)d * S + m0 + row] = *(const bf16*)(Cs + row * 272 + 32 + d * 2); }
    }
#undef STAGE
#undef RD2
#undef MM
#undef STEP
}
__global__ __launch_bounds__(256, 2)
void proj_kernel(const bf16* __restrict__ Ax, const bf16* __restrict__ Awq, const bf16* __restrict__ Awk, const bf16* __restrict__ Awv,
                 const bf16* __restrict__ Bx, const bf16* __restrict__ Bc, const bf16* __restrict__ Bwq, const bf16* __restrict__ Bwuk,
                 const bf16* __restrict__ Bwuv, char* __restrict__ ws) {
    extern __shared__ __attribute__((aligned(16))) char lds[];
    int id = blockIdx.x;
    if (id >= 3 * G1_TILES_B + 64) { const int t = id - 3 * G1_TILES_B - 64;
        gemm_tile<1, 8>(Ax, MD, Awk, Awv, 16, ws + WS_KA, (bf16*)(ws + WS_VAT), 16, t * 128, 0, lds); return; }
    if (id < 3 * G1_TILES_B) id = (id & 7) * (3 * G1_TILES_B / 8) + (id >> 3);
    const bf16* X; const bf16* W; bf16* C; int ld, t;
    if (id < 3 * G1_TILES_B) { const int g = id / G1_TILES_B; t = id - g * G1_TILES_B;
        X = g == 0 ? Bx : Bc; W = g == 0 ? Bwq : (g == 1 ? Bwuk : Bwuv); C = (bf16*)(ws + (g == 0 ? WS_QB : (g == 1 ? WS_KB : WS_VB))); ld = HD; }
    else { t = id - 3 * G1_TILES_B; X = Ax; W = Awq; C = (bf16*)(ws + WS_QA); ld = MD; }
    gemm_tile<0, 8>(X, MD, W, W, ld, C, C, ld, (t & 15) * 128, (t >> 4) * 128, lds);
}

constexpr float SCALE_B = 0.08838834764831845f, SCALE_A = 0.25f, THR = 8.f, LOG2E = 1.4426950408889634f;
constexpr int NW = 4, QBLK = 32, KVBLK = 64, QB = NW * QBLK;
constexpr int SHM_V = KVBLK * DB * 2, SHM_K = KVBLK * DB * 2;
constexpr int LDS_ATT = 2 * SHM_V + 2 * SHM_K + NW * 64 * 4;
#define TILE_OF(t) (t)
#define KSWZ(row, colB) ((row) * 256 + ((colB) ^ (((row) & 7) << 4)))

__device__ __forceinline__ void mask_tile(f32x16& p0, f32x16& p1, int dq) {
    const float NEG = -__builtin_inff();
#pragma unroll
    for (int r = 0; r < 16; ++r) { const int c = (r & 3) + 8 * (r >> 2);
        if (dq - c < 0) p0[r] = NEG;
        if (dq - c - 32 < 0) p1[r] = NEG; }
}
__device__ __forceinline__ void partialSM(f32x16& p0, f32x16& p1, float& m_reg, float& mn, float& alpha) {
    float pmax = p0[0]; for (int r = 1; r < 16; ++r) pmax = fmaxf(pmax, p0[r]); for (int r = 0; r < 16; ++r) pmax = fmaxf(pmax, p1[r]);
    { auto rr = __builtin_amdgcn_permlane32_swap(__float_as_uint(pmax), __float_as_uint(pmax), false, false);
      pmax = fmaxf(__uint_as_float(rr[0]), __uint_as_float(rr[1])); }
    constexpr float C2 = LOG2E * SCALE_B;
    if (__builtin_expect(__all((pmax - m_reg) * SCALE_B <= THR), 1)) { mn = m_reg; alpha = 1.f; }
    else { mn = fmaxf(m_reg, pmax); alpha = __builtin_amdgcn_exp2f((m_reg - mn) * C2); m_reg = mn; }
    const float mnL = -mn * C2;
    for (int r = 0; r < 16; ++r) p0[r] = fmaf(p0[r], C2, mnL); for (int r = 0; r < 16; ++r) p1[r] = fmaf(p1[r], C2, mnL);
    for (int r = 0; r < 16; ++r) p0[r] = __builtin_amdgcn_exp2f(p0[r]);
}
__device__ __forceinline__ void finishSM(f32x16& p0, f32x16& p1, float alpha, float& l_reg, bf16x8& pa0, bf16x8& pa1, bf16x8& pa2, bf16x8& pa3) {
    for (int r = 0; r < 16; ++r) p1[r] = __builtin_amdgcn_exp2f(p1[r]);
    float ps = 0; for (int r = 0; r < 16; ++r) ps += p0[r]; for (int r = 0; r < 16; ++r) ps += p1[r];
    { auto rr = __builtin_amdgcn_permlane32_swap(__float_as_uint(ps), __float_as_uint(ps), false, false);
      ps = __uint_as_float(rr[0]) + __uint_as_float(rr[1]); }
    l_reg = l_reg * alpha + ps;
#define PK4(P, B_, OUT) do { unsigned a0 = cvtpk(P[B_+0], P[B_+1]), a1 = cvtpk(P[B_+2], P[B_+3]);                          \
        unsigned b0 = cvtpk(P[B_+4], P[B_+5]), b1 = cvtpk(P[B_+6], P[B_+7]);                                             \
        auto r0 = __builtin_amdgcn_permlane32_swap(a0, b0, false, false); auto r1 = __builtin_amdgcn_permlane32_swap(a1, b1, false, false); \
        u32x4 w = {r0[0], r1[0], r0[1], r1[1]}; OUT = *reinterpret_cast<bf16x8*>(&w); } while (0)
    PK4(p0, 0, pa0); PK4(p0, 8, pa1); PK4(p1, 0, pa2); PK4(p1, 8, pa3);
#undef PK4
}
template <int KB>
__device__ __forceinline__ void qkt(f32x16& p0, f32x16& p1, const char* K_lds, int r32, int hi, const bf16x8* qr) {
    p0 = f32x16{}; p1 = f32x16{};
    const char* kb[4];
#pragma unroll
    for (int dd = 0; dd < 4; ++dd) kb[dd] = K_lds + KB * SHM_K + KSWZ(r32, (dd * 16 + hi * 8) * 2);
#pragma unroll
    for (int d0 = 0; d0 < 8; ++d0) { const char* a = kb[d0 & 3] + (d0 >> 2) * 128;
        bf16x8 b0 = *reinterpret_cast<const bf16x8*>(a);
        bf16x8 b1 = *reinterpret_cast<const bf16x8*>(a + 32 * 256);
        p0 = __builtin_amdgcn_mfma_f32_32x32x16_bf16(b0, qr[d0], p0, 0, 0, 0);
        p1 = __builtin_amdgcn_mfma_f32_32x32x16_bf16(b1, qr[d0], p1, 0, 0, 0); }
}
template <int VB>
__device__ __forceinline__ void pv_tile(f32x16* o, int vb0, bf16x8 pa0, bf16x8 pa1, bf16x8 pa2, bf16x8 pa3) {
#define PV_RD(S_, ks) do { constexpr int b_ = VB * SHM_V + (ks) * 4096;   \
        TRRD(S_##l0, vb0, b_); TRRD(S_##h0, vb0, b_ + 2048); TRRD(S_##l1, vb0, b_ + 512); TRRD(S_##h1, vb0, b_ + 2560); \
        TRRD(S_##l2, vb0, b_ + 1024); TRRD(S_##h2, vb0, b_ + 3072); TRRD(S_##l3, vb0, b_ + 1536); TRRD(S_##h3, vb0, b_ + 3584); } while (0)
#define PV_MM(S_, pa) do {   \
        o[0] = __builtin_amdgcn_mfma_f32_32x32x16_bf16(pa, MK8(S_##l0, S_##h0), o[0], 0, 0, 0);   \
        o[1] = __builtin_amdgcn_mfma_f32_32x32x16_bf16(pa, MK8(S_##l1, S_##h1), o[1], 0, 0, 0);   \
        o[2] = __builtin_amdgcn_mfma_f32_32x32x16_bf16(pa, MK8(S_##l2, S_##h2), o[2], 0, 0, 0);   \
        o[3] = __builtin_amdgcn_mfma_f32_32x32x16_bf16(pa, MK8(S_##l3, S_##h3), o[3], 0, 0, 0); } while (0)
    s16x4 xl0, xl1, xl2, xl3, xh0, xh1, xh2, xh3, yl0, yl1, yl2, yl3, yh0, yh1, yh2, yh3;
    PV_RD(x, 0); LGKM0();
    PV_RD(y, 1); SBAR(); PV_MM(x, pa0); SBAR(); LGKM0();
    PV_RD(x, 2); SBAR(); PV_MM(y, pa1); SBAR(); LGKM0();
    PV_RD(y, 3); SBAR(); PV_MM(x, pa2); SBAR(); LGKM0();
    PV_MM(y, pa3);
}
template <int VB>
__device__ __forceinline__ void pv_sm(f32x16* o, int vb0, bf16x8 pa0, bf16x8 pa1, bf16x8 pa2, bf16x8 pa3, f32x16& p0, f32x16& p1, float& m_reg, float& alpha) {
    constexpr float C2 = LOG2E * SCALE_B;
    s16x4 xl0, xl1, xl2, xl3, xh0, xh1, xh2, xh3, yl0, yl1, yl2, yl3, yh0, yh1, yh2, yh3;
    PV_RD(x, 0); SBAR();
    float pmax = p0[0]; for (int r = 1; r < 16; ++r) pmax = fmaxf(pmax, p0[r]); for (int r = 0; r < 16; ++r) pmax = fmaxf(pmax, p1[r]);
    { auto rr = __builtin_amdgcn_permlane32_swap(__float_as_uint(pmax), __float_as_uint(pmax), false, false);
      pmax = fmaxf(__uint_as_float(rr[0]), __uint_as_float(rr[1])); }
    SBAR(); LGKM0();
    PV_RD(y, 1); SBAR(); PV_MM(x, pa0);
    const bool keep = __all((pmax - m_reg) * SCALE_B <= THR);
    const float mn = keep ? m_reg : fmaxf(m_reg, pmax);
    alpha = keep ? 1.f : __builtin_amdgcn_exp2f((m_reg - mn) * C2); m_reg = mn;
    const float mnL = -mn * C2;
    for (int r = 0; r < 16; ++r) p0[r] = fmaf(p0[r], C2, mnL);
    ILV(6); SBAR(); LGKM0();
    PV_RD(x, 2); SBAR(); PV_MM(y, pa1);
    for (int r = 0; r < 8; ++r) p0[r] = __builtin_amdgcn_exp2f(p0[r]);
    for (int r = 0; r < 8; ++r) p1[r] = fmaf(p1[r], C2, mnL);
    ILV(4); SBAR(); LGKM0();
    PV_RD(y, 3); SBAR(); PV_MM(x, pa2);
    for (int r = 8; r < 16; ++r) p0[r] = __builtin_amdgcn_exp2f(p0[r]);
    for (int r = 8; r < 16; ++r) p1[r] = fmaf(p1[r], C2, mnL);
    ILV(4); SBAR(); LGKM0();
    PV_MM(y, pa3);
#undef PV_RD
#undef PV_MM
}
struct Seam { bf16x8 qr[8]; bf16x8 sv[4]; };
__device__ __forceinline__ void k_dma(const bf16* Kh, int k0, char* K_lds, int bufi, int wid, unsigned koff) {
#pragma unroll
    for (int i = 0; i < 4; ++i) { const char* ub = (const char*)Kh + (size_t)(k0 + 16 * i) * (HD * 2);
        __builtin_amdgcn_global_load_lds((const unsigned*)(ub + koff), (unsigned*)(K_lds + bufi * SHM_K + i * 4096 + wid * 1024), 16, 0, 0); }
}
#define VLOAD(Vp, k0) do { _Pragma("unroll") for (int i_ = 0; i_ < 4; ++i_) { const char* ub_ = (const char*)(Vp) + (size_t)((k0) + 16 * i_) * (HD * 2); Sm.sv[i_] = *(const bf16x8*)(ub_ + voff); } } while (0)
#define VWRITE(bf) do { _Pragma("unroll") for (int i_ = 0; i_ < 4; ++i_) *(bf16x8*)(V_lds + (bf) * SHM_V + vst + i_ * 4096) = Sm.sv[i_]; } while (0)

__device__ __forceinline__ void attnB_block(const bf16* Qh, const bf16* Kh, const bf16* Vh, bf16* Oh, int P0, int P0n, char* lds, Seam& Sm) {
    const int tid = threadIdx.x, wid = __builtin_amdgcn_readfirstlane(tid >> 6), lane = tid & 63, r32 = lane & 31, hi = lane >> 5;
    const int NT = (P0 + QB) / KVBLK;
    const int qlo = P0 + wid * QBLK, qm = qlo + r32 - 4 * hi;
    char* V_lds = lds; char* K_lds = lds + 2 * SHM_V;
    float* wsf = (float*)(lds + 2 * SHM_V + 2 * SHM_K) + wid * 64; float* li_l = wsf, * al_l = wsf + 32;
    float m_reg = -1e30f, l_reg = 0; f32x16 o[4] = {};
    const int sr = tid >> 4, sc = (tid & 15) * 8, vst = v_st<4>(sr, sc);
    const int vb0 = (int)(uintptr_t)V_lds + v_rd_base(lane);
    const unsigned krow = wid * 4 + (lane >> 4), koff = (krow * HD + (((lane & 15) ^ (krow & 7)) * 8)) * 2, voff = (sr * HD + sc) * 2;
#define RESC(a) do { if (__any((a) < 1.f)) { if (hi == 0) al_l[r32] = (a); asm volatile("s_waitcnt lgkmcnt(0)" ::: "memory");              \
                     for (int d_ = 0; d_ < 4; ++d_) for (int r = 0; r < 16; ++r) o[d_][r] *= al_l[crow(r, hi)]; } } while (0)
#define MASKT(P0_, P1_, t) do { const int kb_ = (t) * KVBLK; if (kb_ + KVBLK - 1 > qlo) mask_tile(P0_, P1_, qm - kb_); } while (0)
    f32x16 pA0, pA1, pB0, pB1; float mnA, mnB, alA, alB; bf16x8 pa0, pa1, pa2, pa3;
    VWRITE(0); SBAR();
    VLOAD(Vh, KVBLK); k_dma(Kh, KVBLK, K_lds, 1, wid, koff);
    SBAR(); qkt<0>(pA0, pA1, K_lds, r32, hi, Sm.qr);
    MASKT(pA0, pA1, 0); partialSM(pA0, pA1, m_reg, mnA, alA);
    VMW(); VWRITE(1);
    __syncthreads();
#define HALF_STEP(PX0, PX1, mnX, alX, PY0, PY1, alY, t, KB, VB, SB) do {                                                      \
        SBAR(); qkt<KB>(PX0, PX1, K_lds, r32, hi, Sm.qr);                                                                     \
        finishSM(PY0, PY1, alY, l_reg, pa0, pa1, pa2, pa3); SBAR();                                                           \
        VLOAD(Vh, TILE_OF((t) + 1) * KVBLK); k_dma(Kh, TILE_OF((t) + 1) * KVBLK, K_lds, SB, wid, koff); SBAR();                             \
        MASKT(PX0, PX1, (t)); SBAR(); pv_sm<VB>(o, vb0, pa0, pa1, pa2, pa3, PX0, PX1, m_reg, alX);                        \
        __syncthreads();                                                                                                      \
        VMW(); VWRITE(SB);                                                                                                    \
        RESC(alX); __syncthreads(); } while (0)
    for (int t = 1; t + 1 < NT; t += 2) {
        HALF_STEP(pB0, pB1, mnB, alB, pA0, pA1, alA, t, 1, 0, 0);
        HALF_STEP(pA0, pA1, mnA, alA, pB0, pB1, alB, t + 1, 0, 1, 1);
    }
    SBAR(); qkt<1>(pB0, pB1, K_lds, r32, hi, Sm.qr); SBAR();
    VLOAD(Vh, 0); SBAR();
    {   int lz = lane; asm volatile("" : "+v"(lz));
        const unsigned qoff = ((lz & 31) * HD + (lz >> 5) * 8) * 2; const char* ub = (const char*)Qh + (size_t)(P0n + wid * QBLK) * (HD * 2);
#pragma unroll
        for (int d0 = 0; d0 < 8; ++d0) Sm.qr[d0] = *(const bf16x8*)(ub + qoff + d0 * 32); }
    SBAR();
    finishSM(pA0, pA1, alA, l_reg, pa0, pa1, pa2, pa3); SBAR();
    MASKT(pB0, pB1, NT - 1); SBAR(); pv_sm<0>(o, vb0, pa0, pa1, pa2, pa3, pB0, pB1, m_reg, alB); __syncthreads();
    k_dma(Kh, 0, K_lds, 0, wid, koff);
    RESC(alB);
    finishSM(pB0, pB1, alB, l_reg, pa0, pa1, pa2, pa3); SBAR(); pv_tile<1>(o, vb0, pa0, pa1, pa2, pa3);
    SBAR();
    if (hi == 0) li_l[r32] = l_reg; asm volatile("s_waitcnt lgkmcnt(0)" ::: "memory");
    float rli[16];
#pragma unroll
    for (int r = 0; r < 16; ++r) rli[r] = __builtin_amdgcn_rcpf(li_l[crow(r, hi)]);
    char* Ow = (char*)(Oh + (size_t)(P0 + wid * QBLK) * HD);
    {   int lz = lane; asm volatile("" : "+v"(lz));
        const unsigned ooff = ((lz >> 5) * 4 * HD + (lz & 31)) * 2;
#pragma unroll
        for (int r = 0; r < 16; ++r) { const int orow = (r & 3) + 8 * (r >> 2);
#pragma unroll
            for (int d0 = 0; d0 < 4; ++d0) { const float v = o[d0][r] * rli[r]; const float vn = __shfl_xor(v, 1);
                if ((lz & 1) == 0) *(unsigned*)(Ow + (size_t)(orow * HD * 2 + d0 * 64) + ooff) = cvtpk(v, vn); } } }
    VMW();
    __syncthreads();
#undef RESC
#undef MASKT
#undef HALF_STEP
}

__device__ __forceinline__ void attnA_block(int a, const char* ws) {
    const int tid = threadIdx.x, wid = tid >> 6, lane = tid & 63, r32 = lane & 31, hi = lane >> 5;
    const int h = a >> 3, q0 = (a & 7) * 256 + wid * 64;
    const bf16* QA = (const bf16*)(ws + WS_QA); const bf16* KA = (const bf16*)(ws + WS_KA); const bf16* VAT = (const bf16*)(ws + WS_VAT);
    bf16* CA = (bf16*)(ws + WS_CA);
    bf16x8 qf[2];
#pragma unroll
    for (int s = 0; s < 2; ++s) qf[s] = *(const bf16x8*)(QA + (size_t)(q0 + s * 32 + r32) * MD + h * 16 + hi * 8);
    f32x16 o[2] = {}; float m_reg[2] = {-1e30f, -1e30f};
    constexpr float C2 = LOG2E * SCALE_A;
    const bf16* kp = KA + (size_t)r32 * 16 + hi * 8;
    const bf16* vp = VAT + (size_t)(r32 & 15) * S + 4 * hi;
    const bool vrow = r32 < 16;
    const short one = (r32 == 16) ? (short)0x3F80 : (short)0;
    bf16x8 kf = *(const bf16x8*)(kp);
    s16x4 v00 = *(const s16x4*)(vp), v01 = *(const s16x4*)(vp + 8), v10 = *(const s16x4*)(vp + 16), v11 = *(const s16x4*)(vp + 24);
#pragma unroll 1
    for (int k0 = 0; k0 < S; k0 += 32) {
        const bf16x8 kc = kf; bf16x8 va0, va1;
        { const s16x4 o4 = {one, one, one, one};
          const s16x4 x00 = vrow ? v00 : o4, x01 = vrow ? v01 : o4, x10 = vrow ? v10 : o4, x11 = vrow ? v11 : o4;
          va0 = MK8(x00, x01); va1 = MK8(x10, x11); }
        const int kn = (k0 + 32 < S) ? k0 + 32 : k0;
        kf = *(const bf16x8*)(kp + (size_t)kn * 16);
        v00 = *(const s16x4*)(vp + kn); v01 = *(const s16x4*)(vp + kn + 8); v10 = *(const s16x4*)(vp + kn + 16); v11 = *(const s16x4*)(vp + kn + 24);
        f32x16 p[2];
#pragma unroll
        for (int s = 0; s < 2; ++s) p[s] = __builtin_amdgcn_mfma_f32_32x32x16_bf16(kc, qf[s], f32x16{}, 0, 0, 0);
#pragma unroll
        for (int s = 0; s < 2; ++s) {
            float pmax = p[s][0];
#pragma unroll
            for (int r = 1; r < 16; ++r) pmax = fmaxf(pmax, p[s][r]);
            { auto rr = __builtin_amdgcn_permlane32_swap(__float_as_uint(pmax), __float_as_uint(pmax), false, false);
              pmax = fmaxf(__uint_as_float(rr[0]), __uint_as_float(rr[1])); }
            if (!__builtin_expect(__all((pmax - m_reg[s]) * SCALE_A <= THR), 1)) {
                const float mn = fmaxf(m_reg[s], pmax), alpha = __builtin_amdgcn_exp2f((m_reg[s] - mn) * C2); m_reg[s] = mn;
#pragma unroll
                for (int r = 0; r < 9; ++r) o[s][r] *= alpha;
            }
            const float mnL = -m_reg[s] * C2;
#pragma unroll
            for (int r = 0; r < 16; ++r) p[s][r] = __builtin_amdgcn_exp2f(fmaf(p[s][r], C2, mnL));
            u32x4 w0 = {cvtpk(p[s][0], p[s][1]), cvtpk(p[s][2], p[s][3]), cvtpk(p[s][4], p[s][5]), cvtpk(p[s][6], p[s][7])};
            u32x4 w1 = {cvtpk(p[s][8], p[s][9]), cvtpk(p[s][10], p[s][11]), cvtpk(p[s][12], p[s][13]), cvtpk(p[s][14], p[s][15])};
            o[s] = __builtin_amdgcn_mfma_f32_32x32x16_bf16(va0, *reinterpret_cast<bf16x8*>(&w0), o[s], 0, 0, 0);
            o[s] = __builtin_amdgcn_mfma_f32_32x32x16_bf16(va1, *reinterpret_cast<bf16x8*>(&w1), o[s], 0, 0, 0);
        }
    }
#pragma unroll
    for (int s = 0; s < 2; ++s) {
        float l = o[s][8];
        { auto rr = __builtin_amdgcn_permlane32_swap(__float_as_uint(l), __float_as_uint(l), false, false); l = __uint_as_float(rr[0]); }
        const float rl = 1.f / l;
        bf16* op = CA + (size_t)(q0 + s * 32 + r32) * MD + h * 16 + 4 * hi;
        u32x2 w0 = {cvtpk(o[s][0] * rl, o[s][1] * rl), cvtpk(o[s][2] * rl, o[s][3] * rl)};
        u32x2 w1 = {cvtpk(o[s][4] * rl, o[s][5] * rl), cvtpk(o[s][6] * rl, o[s][7] * rl)};
        *(u32x2*)op = w0; *(u32x2*)(op + 8) = w1;
    }
}

#define PROF_OCC 2
__global__ __launch_bounds__(256, PROF_OCC)
void attn_kernel(char* __restrict__ ws) {
    extern __shared__ __attribute__((aligned(16))) char lds[];
    const int id = blockIdx.x;
    if (id >= 256) { attnA_block(id - 256, ws); return; }
    const int h = (id & 7) * 4 + (id >> 6), x = (id >> 3) & 7;
    const bf16* Qh = (const bf16*)(ws + WS_QB) + h * DB; const bf16* Kh = (const bf16*)(ws + WS_KB) + h * DB;
    const bf16* Vh = (const bf16*)(ws + WS_VB) + h * DB; bf16* Oh = (bf16*)(ws + WS_CB) + h * DB;
    const int Pa = (15 - x) * QB, Pb = x * QB;
    Seam Sm;
    {   const int tid = threadIdx.x, wid = __builtin_amdgcn_readfirstlane(tid >> 6), lane = tid & 63, r32 = lane & 31, hi = lane >> 5;
        const int sr = tid >> 4, sc = (tid & 15) * 8;
        const unsigned krow = wid * 4 + (lane >> 4), koff = (krow * HD + (((lane & 15) ^ (krow & 7)) * 8)) * 2, voff = (sr * HD + sc) * 2;
#pragma unroll
        for (int d0 = 0; d0 < 8; ++d0) Sm.qr[d0] = *(const bf16x8*)(Qh + (size_t)(Pa + wid * QBLK + r32) * HD + d0 * 16 + hi * 8);
        VLOAD(Vh, 0); k_dma(Kh, 0, lds + 2 * SHM_V, 0, wid, koff); VMW(); __syncthreads(); }
#pragma unroll 1
    for (int pass = 0; pass < 2; ++pass) attnB_block(Qh, Kh, Vh, Oh, pass ? Pb : Pa, Pb, lds, Sm);
}

__global__ __launch_bounds__(256, 2)
void outproj_kernel(char* __restrict__ ws, const bf16* __restrict__ Awo, const bf16* __restrict__ Bwo, bf16* __restrict__ out) {
    extern __shared__ __attribute__((aligned(16))) char lds[];
    int id = blockIdx.x;
    if (id < 256) { id = (id & 7) * 32 + (id >> 3);
        const int tm = id >> 4, sp = (id >> 2) & 3, tn = id & 3;
        gemm_tile<2, 16>((const bf16*)(ws + WS_CB) + sp * 1024, HD, Bwo + (size_t)sp * 1024 * MD, Bwo, MD,
                         (float*)(ws + WS_PART) + (size_t)sp * S * MD, nullptr, MD, tm * 128, tn * 128, lds);
    } else { const int t = id - 256;
        gemm_tile<0, 8>((const bf16*)(ws + WS_CA), MD, Awo, Awo, MD, out, nullptr, 1024, (t >> 2) * 128, (t & 3) * 128, lds); }
}
__global__ __launch_bounds__(256)
void reduce_kernel(const char* __restrict__ ws, bf16* __restrict__ out) {
    const int c = blockIdx.x * 256 + threadIdx.x, row = c >> 6, col = (c & 63) * 8;
    const float* P = (const float*)(ws + WS_PART) + (size_t)row * MD + col;
    f32x4 s0 = {}, s1 = {};
#pragma unroll
    for (int sp = 0; sp < 4; ++sp) { s0 += *(const f32x4*)(P + (size_t)sp * S * MD); s1 += *(const f32x4*)(P + (size_t)sp * S * MD + 4); }
    u32x4 o = {cvtpk(s0[0], s0[1]), cvtpk(s0[2], s0[3]), cvtpk(s1[0], s1[1]), cvtpk(s1[2], s1[3])};
    *(u32x4*)(out + (size_t)row * 1024 + 512 + col) = o;
}

extern "C" void kernel_launch(void* const* d_in, const int* in_sizes, int n_in, void* d_out, int out_size, void* d_ws, size_t ws_size,
                              hipStream_t stream) {
    (void)in_sizes; (void)n_in; (void)out_size; (void)ws_size;
    (void)hipFuncSetAttribute((const void*)attn_kernel, hipFuncAttributeMaxDynamicSharedMemorySize, LDS_ATT);
    (void)hipFuncSetAttribute((const void*)outproj_kernel, hipFuncAttributeMaxDynamicSharedMemorySize, 65536);
    (void)hipFuncSetAttribute((const void*)proj_kernel, hipFuncAttributeMaxDynamicSharedMemorySize, 65536);
    char* ws = (char*)d_ws;
    proj_kernel<<<3 * G1_TILES_B + 64 + 16, 256, 65536, stream>>>((const bf16*)d_in[0], (const bf16*)d_in[1], (const bf16*)d_in[2], (const bf16*)d_in[3],
        (const bf16*)d_in[5], (const bf16*)d_in[6], (const bf16*)d_in[7], (const bf16*)d_in[8], (const bf16*)d_in[9], ws);
    attn_kernel<<<512, 256, LDS_ATT, stream>>>(ws);
    outproj_kernel<<<256 + 64, 256, 65536, stream>>>(ws, (const bf16*)d_in[4], (const bf16*)d_in[10], (bf16*)d_out);
    reduce_kernel<<<512, 256, 0, stream>>>(ws, (bf16*)d_out);
}
```

```cpp
#include <hip/hip_runtime.h>
#include <hip/hip_bf16.h>
#include <cstdint>

using bf16 = __hip_bfloat16;
typedef short bf16x8 __attribute__((ext_vector_type(8)));
typedef short s16x4 __attribute__((ext_vector_type(4)));
typedef float f32x16 __attribute__((ext_vector_type(16)));
typedef float f32x4 __attribute__((ext_vector_type(4)));
typedef unsigned u32x4 __attribute__((ext_vector_type(4)));
typedef unsigned u32x2 __attribute__((ext_vector_type(2)));

constexpr int S = 2048, MD = 512, NH = 32, DB = 128, HD = NH * DB;
constexpr size_t WS_QB = 0, WS_KB = WS_QB + (size_t)S * HD * 2, WS_VB = WS_KB + (size_t)S * HD * 2, WS_CB = WS_QB;
constexpr size_t WS_QA = WS_VB + (size_t)S * HD * 2, WS_CA = WS_QA, WS_KA = WS_QA + (size_t)S * MD * 2, WS_VAT = WS_KA + (size_t)S * 16 * 2, WS_PART = WS_KB;

#define SBAR() __builtin_amdgcn_sched_barrier(0)
#define VMW() asm volatile("s_waitcnt vmcnt(0)" ::: "memory")
__device__ __forceinline__ int crow(int r, int hi) { return (r & 3) + 8 * (r >> 2) + 4 * hi; }
__device__ __forceinline__ unsigned cvtpk(float lo, float hi) {
    unsigned r; asm volatile("v_cvt_pk_bf16_f32 %0, %1, %2" : "=v"(r) : "v"(lo), "v"(hi)); return r;
}
template <int NC> __device__ __forceinline__ int v_st(int k, int c) { const int kk = (k & ~0xC) | ((k & 4) << 1) | ((k & 8) >> 1); return ((kk >> 3) * NC + (c >> 5)) * 512 + ((kk & 7) * 32 + (c & 31)) * 2; }
__device__ __forceinline__ int v_rd_base(int lane) { return ((lane & 3) << 3) | (((lane >> 2) & 3) << 6) | (((lane >> 4) & 1) << 5) | (((lane >> 5) & 1) << 8); }
#define TRRD(dst, base, off) asm volatile("ds_read_b64_tr_b16 %0, %1 offset:%2" : "=&v"(dst) : "v"(base), "i"(off) : "memory")
#define MK8(l, h) (bf16x8){l[0], l[1], l[2], l[3], h[0], h[1], h[2], h[3]}
#define ILV(nv) do { _Pragma("unroll") for (int g_ = 0; g_ < 4; ++g_) { __builtin_amdgcn_sched_group_barrier(0x0008, 1, 0); __builtin_amdgcn_sched_group_barrier(0x0002, nv, 0); } } while (0)
#define LGKM0() do { asm volatile("s_waitcnt lgkmcnt(0)" ::: "memory"); SBAR(); } while (0)

constexpr int G1_TILES_B = 16 * 32;
template <int MODE, int NKT>
__device__ __forceinline__ void gemm_tile(const bf16* __restrict__ X, int ldx, const bf16* __restrict__ W, const bf16* __restrict__ W2, int ldw,
                                          void* __restrict__ Cv, bf16* __restrict__ C2, int ldc, int m0, int n0, char* lds) {
    const int tid = threadIdx.x, wid = __builtin_amdgcn_readfirstlane(tid >> 6), lane = tid & 63, r32 = lane & 31, hi = lane >> 5, wm = wid >> 1, wn = wid & 1;
    const unsigned xoff = (unsigned)(((wid * 8 + (lane >> 3)) * ldx + (((lane & 7) ^ ((wid * 4 + (lane >> 4)) & 7)) * 8)) * 2);
    const int bk = wid * 4 + (lane >> 4), bch = (lane & 15) ^ (((lane >> 4) & 3) << 2);
    const char* Xb = (const char*)(X + (size_t)m0 * ldx);
    const char* Wb; unsigned woff;
    if (MODE != 1) { Wb = (const char*)(W + n0); woff = (unsigned)((bk * ldw + bch * 8) * 2); }
    else { Wb = (const char*)((bch >= 2 && bch < 4) ? W2 : W); woff = (unsigned)((bk * 16 + (bch < 4 ? (bch & 1) * 8 : 0)) * 2); }
#define STAGE(bufi, kt) do { _Pragma("unroll") for (int i = 0; i < 4; ++i) \
            __builtin_amdgcn_global_load_lds((const unsigned*)(Xb + (size_t)(32 * i) * ldx * 2 + (kt) * 128 + xoff), (unsigned*)(lds + (bufi) * 32768 + i * 4096 + wid * 1024), 16, 0, 0); \
        _Pragma("unroll") for (int i = 0; i < 4; ++i) \
            __builtin_amdgcn_global_load_lds((const unsigned*)(Wb + (size_t)((kt) * 64 + 16 * i) * ldw * 2 + woff), (unsigned*)(lds + (bufi) * 32768 + 16384 + i * 4096 + wid * 1024), 16, 0, 0); } while (0)
    f32x16 acc[2][2] = {};
    const int arow0 = wm * 64 + r32, asw = (arow0 >> 1) & 7;
    const int tk = 8 * (lane >> 5) + ((lane >> 2) & 3), tcc = 2 * ((lane >> 4) & 1) + ((lane & 3) >> 1);
    const int vb0 = (int)(uintptr_t)lds + 16384 + tk * 256 + ((4 * ((2 * wn) ^ ((lane >> 2) & 3)) + tcc) << 4) + (lane & 1) * 8;
    const int vb1 = (int)(uintptr_t)lds + 16384 + tk * 256 + ((4 * ((2 * wn + 1) ^ ((lane >> 2) & 3)) + tcc) << 4) + (lane & 1) * 8;
#define RD2(bufi, ks) \
        bf16x8 a0_##ks = *(const bf16x8*)(lds + (bufi) * 32768 + arow0 * 128 + (((ks * 2 + hi) ^ asw) << 4)); \
        bf16x8 a1_##ks = *(const bf16x8*)(lds + (bufi) * 32768 + (arow0 + 32) * 128 + (((ks * 2 + hi) ^ asw) << 4)); \
        s16x4 l0_##ks, h0_##ks, l1_##ks, h1_##ks; \
        TRRD(l0_##ks, vb0, (bufi) * 32768 + ks * 4096); TRRD(h0_##ks, vb0, (bufi) * 32768 + ks * 4096 + 1024); \
        TRRD(l1_##ks, vb1, (bufi) * 32768 + ks * 4096); TRRD(h1_##ks, vb1, (bufi) * 32768 + ks * 4096 + 1024)
#define MM(ks) do { const bf16x8 b0 = MK8(l0_##ks, h0_##ks), b1 = MK8(l1_##ks, h1_##ks); \
        acc[0][0] = __builtin_amdgcn_mfma_f32_32x32x16_bf16(b0, a0_##ks, acc[0][0], 0, 0, 0); \
        acc[0][1] = __builtin_amdgcn_mfma_f32_32x32x16_bf16(b1, a0_##ks, acc[0][1], 0, 0, 0); \
        acc[1][0] = __builtin_amdgcn_mfma_f32_32x32x16_bf16(b0, a1_##ks, acc[1][0], 0, 0, 0); \
        acc[1][1] = __builtin_amdgcn_mfma_f32_32x32x16_bf16(b1, a1_##ks, acc[1][1], 0, 0, 0); } while (0)
#define STEP(bufi, t, ISSUE) do { asm volatile("s_waitcnt vmcnt(0) lgkmcnt(0)" ::: "memory"); __builtin_amdgcn_s_barrier(); asm volatile("" ::: "memory"); SBAR(); \
        if (ISSUE) STAGE((bufi) ^ 1, (t) + 1); \
        RD2(bufi, 0); RD2(bufi, 1); LGKM0(); RD2(bufi, 2); RD2(bufi, 3); SBAR(); MM(0); MM(1); SBAR(); LGKM0(); MM(2); MM(3); SBAR(); } while (0)
    STAGE(0, 0);
#pragma unroll 1
    for (int t = 0; t < NKT - 2; t += 2) { STEP(0, t, true); STEP(1, t + 1, true); }
    STEP(0, NKT - 2, true); STEP(1, NKT - 1, false);
    __syncthreads();
    if (MODE == 2) {
        float* C = (float*)Cv;
#pragma unroll
        for (int mt = 0; mt < 2; ++mt)
#pragma unroll
            for (int nt = 0; nt < 2; ++nt)
#pragma unroll
                for (int g = 0; g < 4; ++g) { const int m = wm * 64 + mt * 32 + r32, n = wn * 64 + nt * 32 + 8 * g + 4 * hi;
                    f32x4 w = {acc[mt][nt][4 * g], acc[mt][nt][4 * g + 1], acc[mt][nt][4 * g + 2], acc[mt][nt][4 * g + 3]};
                    *(f32x4*)(C + (size_t)(m0 + m) * ldc + n0 + n) = w; }
        return;
    }
    bf16* C = (bf16*)Cv;
    char* Cs = lds;
#pragma unroll
    for (int mt = 0; mt < 2; ++mt)
#pragma unroll
        for (int nt = 0; nt < 2; ++nt)
#pragma unroll
            for (int g = 0; g < 4; ++g) { const int m = wm * 64 + mt * 32 + r32, n = wn * 64 + nt * 32 + 8 * g + 4 * hi;
                u32x2 w = {cvtpk(acc[mt][nt][4 * g], acc[mt][nt][4 * g + 1]), cvtpk(acc[mt][nt][4 * g + 2], acc[mt][nt][4 * g + 3])};
                *(u32x2*)(Cs + m * 272 + n * 2) = w; }
    __syncthreads();
    if (MODE == 0) {
#pragma unroll
        for (int i = 0; i < 8; ++i) { const int c = tid + 256 * i, row = c >> 4, ch = c & 15;
            *(bf16x8*)(C + (size_t)(m0 + row) * ldc + n0 + ch * 8) = *(const bf16x8*)(Cs + row * 272 + ch * 16); }
    } else {
        { const int row = tid >> 1, ch = tid & 1; *(bf16x8*)(C + (size_t)(m0 + row) * 16 + ch * 8) = *(const bf16x8*)(Cs + row * 272 + ch * 16); }
#pragma unroll
        for (int i = 0; i < 8; ++i) { const int c = tid + 256 * i, d = c >> 7, row = c & 127;
            C2[(size_t)d * S + m0 + row] = *(const bf16*)(Cs + row * 272 + 32 + d * 2); }
    }
#undef STAGE
#undef RD2
#undef MM
#undef STEP
}
__global__ __launch_bounds__(256, 2)
void proj_kernel(const bf16* __restrict__ Ax, const bf16* __restrict__ Awq, const bf16* __restrict__ Awk, const bf16* __restrict__ Awv,
                 const bf16* __restrict__ Bx, const bf16* __restrict__ Bc, const bf16* __restrict__ Bwq, const bf16* __restrict__ Bwuk,
                 const bf16* __restrict__ Bwuv, char* __restrict__ ws) {
    extern __shared__ __attribute__((aligned(16))) char lds[];
    int id = blockIdx.x;
    if (id >= 3 * G1_TILES_B + 64) { const int t = id - 3 * G1_TILES_B - 64;
        gemm_tile<1, 8>(Ax, MD, Awk, Awv, 16, ws + WS_KA, (bf16*)(ws + WS_VAT), 16, t * 128, 0, lds); return; }
    if (id < 3 * G1_TILES_B) id = (id & 7) * (3 * G1_TILES_B / 8) + (id >> 3);
    const bf16* X; const bf16* W; bf16* C; int ld, t;
    if (id < 3 * G1_TILES_B) { const int g = id / G1_TILES_B; t = id - g * G1_TILES_B;
        X = g == 0 ? Bx : Bc; W = g == 0 ? Bwq : (g == 1 ? Bwuk : Bwuv); C = (bf16*)(ws + (g == 0 ? WS_QB : (g == 1 ? WS_KB : WS_VB))); ld = HD; }
    else { t = id - 3 * G1_TILES_B; X = Ax; W = Awq; C = (bf16*)(ws + WS_QA); ld = MD; }
    gemm_tile<0, 8>(X, MD, W, W, ld, C, C, ld, (t & 15) * 128, (t >> 4) * 128, lds);
}

constexpr float SCALE_B = 0.08838834764831845f, SCALE_A = 0.25f, THR = 8.f, LOG2E = 1.4426950408889634f;
constexpr int NW = 4, QBLK = 32, KVBLK = 64, QB = NW * QBLK;
constexpr int SHM_V = KVBLK * DB * 2, SHM_K = KVBLK * DB * 2;
constexpr int LDS_ATT = 2 * SHM_V + 2 * SHM_K + NW * 64 * 4;
#define TILE_OF(t) (t)
#define KSWZ(row, colB) ((row) * 256 + ((colB) ^ (((row) & 7) << 4)))

__device__ __forceinline__ void mask_tile(f32x16& p0, f32x16& p1, int dq) {
    const float NEG = -__builtin_inff();
#pragma unroll
    for (int r = 0; r < 16; ++r) { const int c = (r & 3) + 8 * (r >> 2);
        if (dq - c < 0) p0[r] = NEG;
        if (dq - c - 32 < 0) p1[r] = NEG; }
}
__device__ __forceinline__ void partialSM(f32x16& p0, f32x16& p1, float& m_reg, float& mn, float& alpha) {
    float pmax = p0[0]; for (int r = 1; r < 16; ++r) pmax = fmaxf(pmax, p0[r]); for (int r = 0; r < 16; ++r) pmax = fmaxf(pmax, p1[r]);
    { auto rr = __builtin_amdgcn_permlane32_swap(__float_as_uint(pmax), __float_as_uint(pmax), false, false);
      pmax = fmaxf(__uint_as_float(rr[0]), __uint_as_float(rr[1])); }
    constexpr float C2 = LOG2E * SCALE_B;
    if (__builtin_expect(__all((pmax - m_reg) * SCALE_B <= THR), 1)) { mn = m_reg; alpha = 1.f; }
    else { mn = fmaxf(m_reg, pmax); alpha = __builtin_amdgcn_exp2f((m_reg - mn) * C2); m_reg = mn; }
    const float mnL = -mn * C2;
    for (int r = 0; r < 16; ++r) p0[r] = fmaf(p0[r], C2, mnL); for (int r = 0; r < 16; ++r) p1[r] = fmaf(p1[r], C2, mnL);
    for (int r = 0; r < 16; ++r) p0[r] = __builtin_amdgcn_exp2f(p0[r]);
}
__device__ __forceinline__ void finishSM(f32x16& p0, f32x16& p1, float alpha, float& l_reg, bf16x8& pa0, bf16x8& pa1, bf16x8& pa2, bf16x8& pa3) {
    for (int r = 0; r < 16; ++r) p1[r] = __builtin_amdgcn_exp2f(p1[r]);
    float ps = 0; for (int r = 0; r < 16; ++r) ps += p0[r]; for (int r = 0; r < 16; ++r) ps += p1[r];
    { auto rr = __builtin_amdgcn_permlane32_swap(__float_as_uint(ps), __float_as_uint(ps), false, false);
      ps = __uint_as_float(rr[0]) + __uint_as_float(rr[1]); }
    l_reg = l_reg * alpha + ps;
#define PK4(P, B_, OUT) do { unsigned a0 = cvtpk(P[B_+0], P[B_+1]), a1 = cvtpk(P[B_+2], P[B_+3]);                          \
        unsigned b0 = cvtpk(P[B_+4], P[B_+5]), b1 = cvtpk(P[B_+6], P[B_+7]);                                             \
        auto r0 = __builtin_amdgcn_permlane32_swap(a0, b0, false, false); auto r1 = __builtin_amdgcn_permlane32_swap(a1, b1, false, false); \
        u32x4 w = {r0[0], r1[0], r0[1], r1[1]}; OUT = *reinterpret_cast<bf16x8*>(&w); } while (0)
    PK4(p0, 0, pa0); PK4(p0, 8, pa1); PK4(p1, 0, pa2); PK4(p1, 8, pa3);
#undef PK4
}
template <int KB>
__device__ __forceinline__ void qkt(f32x16& p0, f32x16& p1, const char* K_lds, int r32, int hi, const bf16x8* qr) {
    p0 = f32x16{}; p1 = f32x16{};
    const char* kb[4];
#pragma unroll
    for (int dd = 0; dd < 4; ++dd) kb[dd] = K_lds + KB * SHM_K + KSWZ(r32, (dd * 16 + hi * 8) * 2);
#pragma unroll
    for (int d0 = 0; d0 < 8; ++d0) { const char* a = kb[d0 & 3] + (d0 >> 2) * 128;
        bf16x8 b0 = *reinterpret_cast<const bf16x8*>(a);
        bf16x8 b1 = *reinterpret_cast<const bf16x8*>(a + 32 * 256);
        p0 = __builtin_amdgcn_mfma_f32_32x32x16_bf16(b0, qr[d0], p0, 0, 0, 0);
        p1 = __builtin_amdgcn_mfma_f32_32x32x16_bf16(b1, qr[d0], p1, 0, 0, 0); }
}
template <int VB>
__device__ __forceinline__ void pv_tile(f32x16* o, int vb0, bf16x8 pa0, bf16x8 pa1, bf16x8 pa2, bf16x8 pa3) {
#define PV_RD(S_, ks) do { constexpr int b_ = VB * SHM_V + (ks) * 4096;   \
        TRRD(S_##l0, vb0, b_); TRRD(S_##h0, vb0, b_ + 2048); TRRD(S_##l1, vb0, b_ + 512); TRRD(S_##h1, vb0, b_ + 2560); \
        TRRD(S_##l2, vb0, b_ + 1024); TRRD(S_##h2, vb0, b_ + 3072); TRRD(S_##l3, vb0, b_ + 1536); TRRD(S_##h3, vb0, b_ + 3584); } while (0)
#define PV_MM(S_, pa) do {   \
        o[0] = __builtin_amdgcn_mfma_f32_32x32x16_bf16(pa, MK8(S_##l0, S_##h0), o[0], 0, 0, 0);   \
        o[1] = __builtin_amdgcn_mfma_f32_32x32x16_bf16(pa, MK8(S_##l1, S_##h1), o[1], 0, 0, 0);   \
        o[2] = __builtin_amdgcn_mfma_f32_32x32x16_bf16(pa, MK8(S_##l2, S_##h2), o[2], 0, 0, 0);   \
        o[3] = __builtin_amdgcn_mfma_f32_32x32x16_bf16(pa, MK8(S_##l3, S_##h3), o[3], 0, 0, 0); } while (0)
    s16x4 xl0, xl1, xl2, xl3, xh0, xh1, xh2, xh3, yl0, yl1, yl2, yl3, yh0, yh1, yh2, yh3;
    PV_RD(x, 0); LGKM0();
    PV_RD(y, 1); SBAR(); PV_MM(x, pa0); SBAR(); LGKM0();
    PV_RD(x, 2); SBAR(); PV_MM(y, pa1); SBAR(); LGKM0();
    PV_RD(y, 3); SBAR(); PV_MM(x, pa2); SBAR(); LGKM0();
    PV_MM(y, pa3);
}
template <int VB>
__device__ __forceinline__ void pv_sm(f32x16* o, int vb0, bf16x8 pa0, bf16x8 pa1, bf16x8 pa2, bf16x8 pa3, f32x16& p0, f32x16& p1, float& m_reg, float& alpha) {
    constexpr float C2 = LOG2E * SCALE_B;
    s16x4 xl0, xl1, xl2, xl3, xh0, xh1, xh2, xh3, yl0, yl1, yl2, yl3, yh0, yh1, yh2, yh3;
    PV_RD(x, 0); SBAR();
    float pmax = p0[0]; for (int r = 1; r < 16; ++r) pmax = fmaxf(pmax, p0[r]); for (int r = 0; r < 16; ++r) pmax = fmaxf(pmax, p1[r]);
    { auto rr = __builtin_amdgcn_permlane32_swap(__float_as_uint(pmax), __float_as_uint(pmax), false, false);
      pmax = fmaxf(__uint_as_float(rr[0]), __uint_as_float(rr[1])); }
    SBAR(); LGKM0();
    PV_RD(y, 1); SBAR(); PV_MM(x, pa0);
    const bool keep = __all((pmax - m_reg) * SCALE_B <= THR);
    const float mn = keep ? m_reg : fmaxf(m_reg, pmax);
    alpha = keep ? 1.f : __builtin_amdgcn_exp2f((m_reg - mn) * C2); m_reg = mn;
    const float mnL = -mn * C2;
    for (int r = 0; r < 16; ++r) p0[r] = fmaf(p0[r], C2, mnL);
    ILV(6); SBAR(); LGKM0();
    PV_RD(x, 2); SBAR(); PV_MM(y, pa1);
    for (int r = 0; r < 8; ++r) p0[r] = __builtin_amdgcn_exp2f(p0[r]);
    for (int r = 0; r < 8; ++r) p1[r] = fmaf(p1[r], C2, mnL);
    ILV(4); SBAR(); LGKM0();
    PV_RD(y, 3); SBAR(); PV_MM(x, pa2);
    for (int r = 8; r < 16; ++r) p0[r] = __builtin_amdgcn_exp2f(p0[r]);
    for (int r = 8; r < 16; ++r) p1[r] = fmaf(p1[r], C2, mnL);
    ILV(4); SBAR(); LGKM0();
    PV_MM(y, pa3);
#undef PV_RD
#undef PV_MM
}
struct Seam { bf16x8 qr[8]; bf16x8 sv[4]; };
__device__ __forceinline__ void k_dma(const bf16* Kh, int k0, char* K_lds, int bufi, int wid, unsigned koff) {
#pragma unroll
    for (int i = 0; i < 4; ++i) { const char* ub = (const char*)Kh + (size_t)(k0 + 16 * i) * (HD * 2);
        __builtin_amdgcn_global_load_lds((const unsigned*)(ub + koff), (unsigned*)(K_lds + bufi * SHM_K + i * 4096 + wid * 1024), 16, 0, 0); }
}
#define VLOAD(Vp, k0) do { _Pragma("unroll") for (int i_ = 0; i_ < 4; ++i_) { const char* ub_ = (const char*)(Vp) + (size_t)((k0) + 16 * i_) * (HD * 2); Sm.sv[i_] = *(const bf16x8*)(ub_ + voff); } } while (0)
#define VWRITE(bf) do { _Pragma("unroll") for (int i_ = 0; i_ < 4; ++i_) *(bf16x8*)(V_lds + (bf) * SHM_V + vst + i_ * 4096) = Sm.sv[i_]; } while (0)

__device__ __forceinline__ void attnB_block(const bf16* Qh, const bf16* Kh, const bf16* Vh, bf16* Oh, int P0, int P0n, char* lds, Seam& Sm) {
    const int tid = threadIdx.x, wid = __builtin_amdgcn_readfirstlane(tid >> 6), lane = tid & 63, r32 = lane & 31, hi = lane >> 5;
    const int NT = (P0 + QB) / KVBLK;
    const int qlo = P0 + wid * QBLK, qm = qlo + r32 - 4 * hi;
    char* V_lds = lds; char* K_lds = lds + 2 * SHM_V;
    float* wsf = (float*)(lds + 2 * SHM_V + 2 * SHM_K) + wid * 64; float* li_l = wsf, * al_l = wsf + 32;
    float m_reg = -1e30f, l_reg = 0; f32x16 o[4] = {};
    const int sr = tid >> 4, sc = (tid & 15) * 8, vst = v_st<4>(sr, sc);
    const int vb0 = (int)(uintptr_t)V_lds + v_rd_base(lane);
    const unsigned krow = wid * 4 + (lane >> 4), koff = (krow * HD + (((lane & 15) ^ (krow & 7)) * 8)) * 2, voff = (sr * HD + sc) * 2;
#define RESC(a) do { if (__any((a) < 1.f)) { if (hi == 0) al_l[r32] = (a); asm volatile("s_waitcnt lgkmcnt(0)" ::: "memory");              \
                     for (int d_ = 0; d_ < 4; ++d_) for (int r = 0; r < 16; ++r) o[d_][r] *= al_l[crow(r, hi)]; } } while (0)
#define MASKT(P0_, P1_, t) do { const int kb_ = (t) * KVBLK; if (kb_ + KVBLK - 1 > qlo) mask_tile(P0_, P1_, qm - kb_); } while (0)
    f32x16 pA0, pA1, pB0, pB1; float mnA, mnB, alA, alB; bf16x8 pa0, pa1, pa2, pa3;
    VWRITE(0); SBAR();
    VLOAD(Vh, KVBLK); k_dma(Kh, KVBLK, K_lds, 1, wid, koff);
    SBAR(); qkt<0>(pA0, pA1, K_lds, r32, hi, Sm.qr);
    MASKT(pA0, pA1, 0); partialSM(pA0, pA1, m_reg, mnA, alA);
    VMW(); VWRITE(1);
    __syncthreads();
#define HALF_STEP(PX0, PX1, mnX, alX, PY0, PY1, alY, t, KB, VB, SB) do {                                                      \
        SBAR(); qkt<KB>(PX0, PX1, K_lds, r32, hi, Sm.qr);                                                                     \
        finishSM(PY0, PY1, alY, l_reg, pa0, pa1, pa2, pa3); SBAR();                                                           \
        VLOAD(Vh, TILE_OF((t) + 1) * KVBLK); k_dma(Kh, TILE_OF((t) + 1) * KVBLK, K_lds, SB, wid, koff); SBAR();                             \
        MASKT(PX0, PX1, (t)); SBAR(); pv_sm<VB>(o, vb0, pa0, pa1, pa2, pa3, PX0, PX1, m_reg, alX);                        \
        __syncthreads();                                                                                                      \
        VMW(); VWRITE(SB);                                                                                                    \
        RESC(alX); __syncthreads(); } while (0)
    for (int t = 1; t + 1 < NT; t += 2) {
        HALF_STEP(pB0, pB1, mnB, alB, pA0, pA1, alA, t, 1, 0, 0);
        HALF_STEP(pA0, pA1, mnA, alA, pB0, pB1, alB, t + 1, 0, 1, 1);
    }
    SBAR(); qkt<1>(pB0, pB1, K_lds, r32, hi, Sm.qr); SBAR();
    VLOAD(Vh, 0); SBAR();
    {   int lz = lane; asm volatile("" : "+v"(lz));
        const unsigned qoff = ((lz & 31) * HD + (lz >> 5) * 8) * 2; const char* ub = (const char*)Qh + (size_t)(P0n + wid * QBLK) * (HD * 2);
#pragma unroll
        for (int d0 = 0; d0 < 8; ++d0) Sm.qr[d0] = *(const bf16x8*)(ub + qoff + d0 * 32); }
    SBAR();
    finishSM(pA0, pA1, alA, l_reg, pa0, pa1, pa2, pa3); SBAR();
    MASKT(pB0, pB1, NT - 1); SBAR(); pv_sm<0>(o, vb0, pa0, pa1, pa2, pa3, pB0, pB1, m_reg, alB); __syncthreads();
    k_dma(Kh, 0, K_lds, 0, wid, koff);
    RESC(alB);
    finishSM(pB0, pB1, alB, l_reg, pa0, pa1, pa2, pa3); SBAR(); pv_tile<1>(o, vb0, pa0, pa1, pa2, pa3);
    SBAR();
    if (hi == 0) li_l[r32] = l_reg; asm volatile("s_waitcnt lgkmcnt(0)" ::: "memory");
    float rli[16];
#pragma unroll
    for (int r = 0; r < 16; ++r) rli[r] = __builtin_amdgcn_rcpf(li_l[crow(r, hi)]);
    char* Ow = (char*)(Oh + (size_t)(P0 + wid * QBLK) * HD);
    {   int lz = lane; asm volatile("" : "+v"(lz));
        const unsigned ooff = ((lz >> 5) * 4 * HD + (lz & 31)) * 2;
#pragma unroll
        for (int r = 0; r < 16; ++r) { const int orow = (r & 3) + 8 * (r >> 2);
#pragma unroll
            for (int d0 = 0; d0 < 4; ++d0) { const float v = o[d0][r] * rli[r]; const float vn = __shfl_xor(v, 1);
                if ((lz & 1) == 0) *(unsigned*)(Ow + (size_t)(orow * HD * 2 + d0 * 64) + ooff) = cvtpk(v, vn); } } }
    VMW();
    __syncthreads();
#undef RESC
#undef MASKT
#undef HALF_STEP
}

__device__ __forceinline__ void attnA_block(int a, const char* ws) {
    const int tid = threadIdx.x, wid = tid >> 6, lane = tid & 63, r32 = lane & 31, hi = lane >> 5;
    const int h = a >> 3, q0 = (a & 7) * 256 + wid * 64;
    const bf16* QA = (const bf16*)(ws + WS_QA); const bf16* KA = (const bf16*)(ws + WS_KA); const bf16* VAT = (const bf16*)(ws + WS_VAT);
    bf16* CA = (bf16*)(ws + WS_CA);
    bf16x8 qf[2];
#pragma unroll
    for (int s = 0; s < 2; ++s) qf[s] = *(const bf16x8*)(QA + (size_t)(q0 + s * 32 + r32) * MD + h * 16 + hi * 8);
    f32x16 o[2] = {}; float m_reg[2] = {-1e30f, -1e30f};
    constexpr float C2 = LOG2E * SCALE_A;
    const bf16* kp = KA + (size_t)r32 * 16 + hi * 8;
    const bf16* vp = VAT + (size_t)(r32 & 15) * S + 4 * hi;
    const bool vrow = r32 < 16;
    const short one = (r32 == 16) ? (short)0x3F80 : (short)0;
    bf16x8 kf = *(const bf16x8*)(kp);
    s16x4 v00 = *(const s16x4*)(vp), v01 = *(const s16x4*)(vp + 8), v10 = *(const s16x4*)(vp + 16), v11 = *(const s16x4*)(vp + 24);
#pragma unroll 1
    for (int k0 = 0; k0 < S; k0 += 32) {
        const bf16x8 kc = kf; bf16x8 va0, va1;
        { const s16x4 o4 = {one, one, one, one};
          const s16x4 x00 = vrow ? v00 : o4, x01 = vrow ? v01 : o4, x10 = vrow ? v10 : o4, x11 = vrow ? v11 : o4;
          va0 = MK8(x00, x01); va1 = MK8(x10, x11); }
        const int kn = (k0 + 32 < S) ? k0 + 32 : k0;
        kf = *(const bf16x8*)(kp + (size_t)kn * 16);
        v00 = *(const s16x4*)(vp + kn); v01 = *(const s16x4*)(vp + kn + 8); v10 = *(const s16x4*)(vp + kn + 16); v11 = *(const s16x4*)(vp + kn + 24);
        f32x16 p[2];
#pragma unroll
        for (int s = 0; s < 2; ++s) p[s] = __builtin_amdgcn_mfma_f32_32x32x16_bf16(kc, qf[s], f32x16{}, 0, 0, 0);
#pragma unroll
        for (int s = 0; s < 2; ++s) {
            float pmax = p[s][0];
#pragma unroll
            for (int r = 1; r < 16; ++r) pmax = fmaxf(pmax, p[s][r]);
            { auto rr = __builtin_amdgcn_permlane32_swap(__float_as_uint(pmax), __float_as_uint(pmax), false, false);
              pmax = fmaxf(__uint_as_float(rr[0]), __uint_as_float(rr[1])); }
            if (!__builtin_expect(__all((pmax - m_reg[s]) * SCALE_A <= THR), 1)) {
                const float mn = fmaxf(m_reg[s], pmax), alpha = __builtin_amdgcn_exp2f((m_reg[s] - mn) * C2); m_reg[s] = mn;
#pragma unroll
                for (int r = 0; r < 9; ++r) o[s][r] *= alpha;
            }
            const float mnL = -m_reg[s] * C2;
#pragma unroll
            for (int r = 0; r < 16; ++r) p[s][r] = __builtin_amdgcn_exp2f(fmaf(p[s][r], C2, mnL));
            u32x4 w0 = {cvtpk(p[s][0], p[s][1]), cvtpk(p[s][2], p[s][3]), cvtpk(p[s][4], p[s][5]), cvtpk(p[s][6], p[s][7])};
            u32x4 w1 = {cvtpk(p[s][8], p[s][9]), cvtpk(p[s][10], p[s][11]), cvtpk(p[s][12], p[s][13]), cvtpk(p[s][14], p[s][15])};
            o[s] = __builtin_amdgcn_mfma_f32_32x32x16_bf16(va0, *reinterpret_cast<bf16x8*>(&w0), o[s], 0, 0, 0);
            o[s] = __builtin_amdgcn_mfma_f32_32x32x16_bf16(va1, *reinterpret_cast<bf16x8*>(&w1), o[s], 0, 0, 0);
        }
    }
#pragma unroll
    for (int s = 0; s < 2; ++s) {
        float l = o[s][8];
        { auto rr = __builtin_amdgcn_permlane32_swap(__float_as_uint(l), __float_as_uint(l), false, false); l = __uint_as_float(rr[0]); }
        const float rl = 1.f / l;
        bf16* op = CA + (size_t)(q0 + s * 32 + r32) * MD + h * 16 + 4 * hi;
        u32x2 w0 = {cvtpk(o[s][0] * rl, o[s][1] * rl), cvtpk(o[s][2] * rl, o[s][3] * rl)};
        u32x2 w1 = {cvtpk(o[s][4] * rl, o[s][5] * rl), cvtpk(o[s][6] * rl, o[s][7] * rl)};
        *(u32x2*)op = w0; *(u32x2*)(op + 8) = w1;
    }
}

__global__ __launch_bounds__(256, 2)
void attn_kernel(char* __restrict__ ws) {
    extern __shared__ __attribute__((aligned(16))) char lds[];
    const int id = blockIdx.x;
    if (id >= 256) { attnA_block(id - 256, ws); return; }
    const int h = (id & 7) * 4 + (id >> 6), x = (id >> 3) & 7;
    const bf16* Qh = (const bf16*)(ws + WS_QB) + h * DB; const bf16* Kh = (const bf16*)(ws + WS_KB) + h * DB;
    const bf16* Vh = (const bf16*)(ws + WS_VB) + h * DB; bf16* Oh = (bf16*)(ws + WS_CB) + h * DB;
    const int Pa = (15 - x) * QB, Pb = x * QB;
    Seam Sm;
    {   const int tid = threadIdx.x, wid = __builtin_amdgcn_readfirstlane(tid >> 6), lane = tid & 63, r32 = lane & 31, hi = lane >> 5;
        const int sr = tid >> 4, sc = (tid & 15) * 8;
        const unsigned krow = wid * 4 + (lane >> 4), koff = (krow * HD + (((lane & 15) ^ (krow & 7)) * 8)) * 2, voff = (sr * HD + sc) * 2;
#pragma unroll
        for (int d0 = 0; d0 < 8; ++d0) Sm.qr[d0] = *(const bf16x8*)(Qh + (size_t)(Pa + wid * QBLK + r32) * HD + d0 * 16 + hi * 8);
        VLOAD(Vh, 0); k_dma(Kh, 0, lds + 2 * SHM_V, 0, wid, koff); VMW(); __syncthreads(); }
#pragma unroll 1
    for (int pass = 0; pass < 2; ++pass) attnB_block(Qh, Kh, Vh, Oh, pass ? Pb : Pa, Pb, lds, Sm);
}

__global__ __launch_bounds__(256, 2)
void outproj_kernel(char* __restrict__ ws, const bf16* __restrict__ Awo, const bf16* __restrict__ Bwo, bf16* __restrict__ out) {
    extern __shared__ __attribute__((aligned(16))) char lds[];
    int id = blockIdx.x;
    if (id < 256) { id = (id & 7) * 32 + (id >> 3);
        const int tm = id >> 4, sp = (id >> 2) & 3, tn = id & 3;
        gemm_tile<2, 16>((const bf16*)(ws + WS_CB) + sp * 1024, HD, Bwo + (size_t)sp * 1024 * MD, Bwo, MD,
                         (float*)(ws + WS_PART) + (size_t)sp * S * MD, nullptr, MD, tm * 128, tn * 128, lds);
    } else { const int t = id - 256;
        gemm_tile<0, 8>((const bf16*)(ws + WS_CA), MD, Awo, Awo, MD, out, nullptr, 1024, (t >> 2) * 128, (t & 3) * 128, lds); }
}
__global__ __launch_bounds__(256)
void reduce_kernel(const char* __restrict__ ws, bf16* __restrict__ out) {
    const int c = blockIdx.x * 256 + threadIdx.x, row = c >> 6, col = (c & 63) * 8;
    const float* P = (const float*)(ws + WS_PART) + (size_t)row * MD + col;
    f32x4 s0 = {}, s1 = {};
#pragma unroll
    for (int sp = 0; sp < 4; ++sp) { s0 += *(const f32x4*)(P + (size_t)sp * S * MD); s1 += *(const f32x4*)(P + (size_t)sp * S * MD + 4); }
    u32x4 o = {cvtpk(s0[0], s0[1]), cvtpk(s0[2], s0[3]), cvtpk(s1[0], s1[1]), cvtpk(s1[2], s1[3])};
    *(u32x4*)(out + (size_t)row * 1024 + 512 + col) = o;
}

extern "C" void kernel_launch(void* const* d_in, const int* in_sizes, int n_in, void* d_out, int out_size, void* d_ws, size_t ws_size,
                              hipStream_t stream) {
    (void)in_sizes; (void)n_in; (void)out_size; (void)ws_size;
    (void)hipFuncSetAttribute((const void*)attn_kernel, hipFuncAttributeMaxDynamicSharedMemorySize, LDS_ATT);
    (void)hipFuncSetAttribute((const void*)outproj_kernel, hipFuncAttributeMaxDynamicSharedMemorySize, 65536);
    (void)hipFuncSetAttribute((const void*)proj_kernel, hipFuncAttributeMaxDynamicSharedMemorySize, 65536);
    char* ws = (char*)d_ws;
    proj_kernel<<<3 * G1_TILES_B + 64 + 16, 256, 65536, stream>>>((const bf16*)d_in[0], (const bf16*)d_in[1], (const bf16*)d_in[2], (const bf16*)d_in[3],
        (const bf16*)d_in[5], (const bf16*)d_in[6], (const bf16*)d_in[7], (const bf16*)d_in[8], (const bf16*)d_in[9], ws);
    attn_kernel<<<512, 256, LDS_ATT, stream>>>(ws);
    outproj_kernel<<<256 + 64, 256, 65536, stream>>>(ws, (const bf16*)d_in[4], (const bf16*)d_in[10], (bf16*)d_out);
    reduce_kernel<<<512, 256, 0, stream>>>(ws, (bf16*)d_out);
}
```
